```python
import torch

def _softmax(x, axis=-1):
    return torch.softmax(x, dim=axis)
def _gelu(x):
    return torch.nn.functional.gelu(x, approximate="tanh")
def _silu(x):
    return torch.nn.functional.silu(x)
def _sigmoid(x):
    return torch.sigmoid(x)
def _layernorm(x, axis=-1, eps=1e-5):
    m = x.mean(dim=axis, keepdim=True)
    v = x.var(dim=axis, keepdim=True, unbiased=False)
    return (x - m) / torch.sqrt(v + eps)
def _scatter_add(tgt, idx, src, axis=0):
    return tgt.clone().index_add_(axis, idx, src)
def _segment_sum(src, seg, n):
    out = torch.zeros((n,) + src.shape[1:], dtype=src.dtype)
    return out.index_add_(0, seg, src)
def _pad(x, pw):
    flat = tuple(v for pair in reversed(pw) for v in pair)
    return torch.nn.functional.pad(x, flat)

kernel_name = 'sequential_preLN__ssm_conv_s4d__glu_gated_bilinear__prompt'

def setup_inputs():

    _w = (lambda a: (lambda t: t.to(torch.bfloat16) if t.is_floating_point() else t)(torch.as_tensor(a)))
    def _A_setup():
        Bsz, L, D, N = 1, 2048, 576, 128
        return {
            "u": _w((lambda *s: torch.randn(*s, dtype=torch.bfloat16) * 0.05)(Bsz, L, D)),
            "log_A_real": _w((lambda *s: torch.randn(*s, dtype=torch.bfloat16) * 6.4)(D, N) * 0.5),
            "A_imag": _w((lambda *s: torch.randn(*s, dtype=torch.bfloat16) * 0.05)(D, N) * 3.14),
            "C_real": _w((lambda *s: torch.randn(*s, dtype=torch.bfloat16) * 0.05)(D, N) / N ** 0.5),
            "C_imag": _w((lambda *s: torch.randn(*s, dtype=torch.bfloat16) * 0.05)(D, N) / N ** 0.5),
            "log_dt": _w((lambda *s: torch.randn(*s, dtype=torch.bfloat16) * 0.05)(D) * 0.5 - 4.0),
            "D_skip": _w((lambda *s: torch.randn(*s, dtype=torch.bfloat16) * 0.05)(D)),
        }
    _A_in = _A_setup()

    def _B_setup():
        B, S, D, F = 1, 2048, 576, 4608
        return {
            "x": _w((lambda *s: torch.randn(*s, dtype=torch.bfloat16) * 0.05)(B, S, D)),
            "w_gate": _w((lambda *s: torch.randn(*s, dtype=torch.bfloat16) * 0.4)(D, F) * (D ** -0.5)),
            "w_up": _w((lambda *s: torch.randn(*s, dtype=torch.bfloat16) * 0.4)(D, F) * (D ** -0.5)),
            "w_down": _w((lambda *s: torch.randn(*s, dtype=torch.bfloat16) * 0.4)(F, D) * (F ** -0.5)),
        }
    _B_in = _B_setup()

    return {
        "A_u": _A_in["u"],
        "A_log_A_real": _A_in["log_A_real"],
        "A_A_imag": _A_in["A_imag"],
        "A_C_real": _A_in["C_real"],
        "A_C_imag": _A_in["C_imag"],
        "A_log_dt": _A_in["log_dt"],
        "A_D_skip": _A_in["D_skip"],
        "B_x": _B_in["x"],
        "B_w_gate": _B_in["w_gate"],
        "B_w_up": _B_in["w_up"],
        "B_w_down": _B_in["w_down"],
    }

def reference(A_u, A_log_A_real, A_A_imag, A_C_real, A_C_imag, A_log_dt, A_D_skip, B_x, B_w_gate, B_w_up, B_w_down):
    def _A_ref(u, log_A_real, A_imag, C_real, C_imag, log_dt, D_skip):
        L = u.shape[1]
        dt = torch.exp(log_dt)
        A = -torch.exp(log_A_real) + 1j * A_imag
        C = (C_real + 1j * C_imag) * dt[:, None]
        pos = (lambda x, dt: x.to(dt))(torch.arange(L), log_dt.dtype)
        P = torch.exp((A * dt[:, None])[:, :, None] * pos[None, None, :])
        K = 2.0 * torch.einsum("dn,dnl->dl", C, P).real
        L2 = 2 * L
        u_t = (lambda x, *d: x.permute(*d))(u, 0, 2, 1)
        Kf = (lambda x, *a, **k: torch.fft.rfft(x.float(), *a, **k))(K, L2)
        Uf = (lambda x, *a, **k: torch.fft.rfft(x.float(), *a, **k))(u_t, L2)
        y = (lambda x, *a, **k: torch.fft.irfft(x, *a, **k).to(torch.bfloat16))(Uf * Kf[None, :, :], L2)[:, :, :L]
        y = (lambda x, *d: x.permute(*d))(y, 0, 2, 1)
        return y + u * D_skip[None, None, :]

    def _B_ref(x, w_gate, w_up, w_down):
        g = torch.einsum("bsd,df->bsf", x, w_gate)
        u = torch.einsum("bsd,df->bsf", x, w_up)
        h = g * u
        return torch.einsum("bsf,fd->bsd", h, w_down)

    x = A_u
    h = x + _A_ref(_layernorm(x, axis=-1), A_log_A_real, A_A_imag, A_C_real, A_C_imag, A_log_dt, A_D_skip)
    h2 = h + _B_ref(_layernorm(h, axis=-1), B_w_gate, B_w_up, B_w_down)
    return h2
```

```cpp
#include <hip/hip_runtime.h>
#include <hip/hip_bf16.h>
#include <stdint.h>
typedef unsigned short u16;
constexpr int L=2048, D=576, N=128, F=4608;
__device__ __forceinline__ float bf2f(u16 v){ return __uint_as_float(((unsigned)v)<<16); }
__device__ __forceinline__ u16 f2bf(float f){ unsigned u=__float_as_uint(f); u += 0x7FFF + ((u>>16)&1); return (u16)(u>>16); }
__device__ __forceinline__ float rbf(float f){ return bf2f(f2bf(f)); }

__global__ void ln_kernel(const u16* x, u16* o){
  int row=blockIdx.x; int t=threadIdx.x;
  float v[9]; float s=0;
  for(int i=0;i<9;i++){ v[i]=bf2f(x[row*D+t+64*i]); s+=v[i]; }
  for(int m=32;m>=1;m>>=1) s+=__shfl_xor(s,m);
  float mean=s/D; float q=0;
  for(int i=0;i<9;i++){ float d=v[i]-mean; q+=d*d; }
  for(int m=32;m>=1;m>>=1) q+=__shfl_xor(q,m);
  float var=rbf(q/D); float mb=rbf(mean);
  float sd=rbf(sqrtf(rbf(var+1e-5f)));
  for(int i=0;i<9;i++){ o[row*D+t+64*i]=f2bf(rbf(v[i]-mb)/sd); }
}
__global__ void kgen(const u16* logA,const u16* Aim,const u16* Cr,const u16* Ci,const u16* logdt,float* K){
  int d=blockIdx.y; int l=blockIdx.x*256+threadIdx.x;
  float dt=rbf(expf(bf2f(logdt[d])));
  float pos=rbf((float)l);
  float acc=0;
  for(int n=0;n<N;n++){
    float ar=-rbf(expf(bf2f(logA[d*N+n])))*dt; float ai=bf2f(Aim[d*N+n])*dt;
    float cr=bf2f(Cr[d*N+n])*dt, ci=bf2f(Ci[d*N+n])*dt;
    float e=expf(ar*pos); float s,c; sincosf(ai*pos,&s,&c);
    acc+=e*(cr*c-ci*s);
  }
  K[d*L+l]=2.f*acc;
}
__global__ void conv(const u16* x,const u16* u,const float* K,const u16* Dk,u16* h){
  int l=blockIdx.x; int d=threadIdx.x;
  float acc=0;
  for(int j=0;j<=l;j++) acc+=K[d*L+j]*bf2f(u[(l-j)*D+d]);
  float y=rbf(acc); float t=rbf(bf2f(u[l*D+d])*bf2f(Dk[d]));
  h[l*D+d]=f2bf(bf2f(x[l*D+d])+rbf(y+t));
}
__global__ void g1(const u16* xn,const u16* wg,const u16* wu,u16* hh){
  int f=blockIdx.x*256+threadIdx.x; int l0=blockIdx.y*8;
  float g[8]={0},u[8]={0};
  for(int k=0;k<D;k++){ float a=bf2f(wg[k*F+f]), b=bf2f(wu[k*F+f]);
    for(int i=0;i<8;i++){ float xv=bf2f(xn[(l0+i)*D+k]); g[i]+=xv*a; u[i]+=xv*b; } }
  for(int i=0;i<8;i++) hh[(l0+i)*F+f]=f2bf(rbf(g[i])*rbf(u[i]));
}
__global__ void g2(const u16* hh,const u16* wd,const u16* h,u16* o){
  int d=threadIdx.x; int l0=blockIdx.x*4;
  float a[4]={0};
  for(int k=0;k<F;k++){ float w=bf2f(wd[k*D+d]); for(int i=0;i<4;i++) a[i]+=bf2f(hh[(l0+i)*F+k])*w; }
  for(int i=0;i<4;i++) o[(l0+i)*D+d]=f2bf(bf2f(h[(l0+i)*D+d])+rbf(a[i]));
}
extern "C" void kernel_launch(void* const* d_in, const int* in_sizes, int n_in, void* d_out, int out_size, void* d_ws, size_t ws_size, hipStream_t stream) {
  const u16* x=(const u16*)d_in[0];
  char* ws=(char*)d_ws;
  u16* u=(u16*)ws; float* K=(float*)(ws+(4<<20)); u16* h=(u16*)(ws+(12<<20)); u16* xn=(u16*)(ws+(16<<20)); u16* hh=(u16*)(ws+(20<<20));
  ln_kernel<<<L,64,0,stream>>>(x,u);
  kgen<<<dim3(L/256,D),256,0,stream>>>((const u16*)d_in[1],(const u16*)d_in[2],(const u16*)d_in[3],(const u16*)d_in[4],(const u16*)d_in[5],K);
  conv<<<L,D,0,stream>>>(x,u,K,(const u16*)d_in[6],h);
  ln_kernel<<<L,64,0,stream>>>(h,xn);
  g1<<<dim3(F/256,L/8),256,0,stream>>>(xn,(const u16*)d_in[8],(const u16*)d_in[9],hh);
  g2<<<L/4,D,0,stream>>>(hh,(const u16*)d_in[10],h,(u16*)d_out);
}
```

```cpp
#include <hip/hip_runtime.h>
#include <hip/hip_bf16.h>
#include <stdint.h>
typedef unsigned short u16;
constexpr int L=2048, D=576, N=128, F=4608;
__device__ __forceinline__ float bf2f(u16 v){ return __uint_as_float(((unsigned)v)<<16); }
__device__ __forceinline__ u16 f2bf(float f){ unsigned u=__float_as_uint(f); u += 0x7FFF + ((u>>16)&1); return (u16)(u>>16); }
__device__ __forceinline__ float rbf(float f){ return bf2f(f2bf(f)); }

__global__ void ln_kernel(const u16* x, u16* o){
  int row=blockIdx.x; int t=threadIdx.x;
  float v[9]; float s=0;
  for(int i=0;i<9;i++){ v[i]=bf2f(x[row*D+t+64*i]); s+=v[i]; }
  for(int m=32;m>=1;m>>=1) s+=__shfl_xor(s,m);
  float mean=s/D; float q=0;
  for(int i=0;i<9;i++){ float d=v[i]-mean; q+=d*d; }
  for(int m=32;m>=1;m>>=1) q+=__shfl_xor(q,m);
  float var=rbf(q/D); float mb=rbf(mean);
  float sd=rbf(sqrtf(rbf(var+1e-5f)));
  for(int i=0;i<9;i++){ o[row*D+t+64*i]=f2bf(rbf(v[i]-mb)/sd); }
}
__global__ void kgen(const u16* logA,const u16* Aim,const u16* Cr,const u16* Ci,const u16* logdt,float* K){
  int d=blockIdx.y; int l=blockIdx.x*256+threadIdx.x;
  float dt=rbf(expf(bf2f(logdt[d])));
  float pos=rbf((float)l);
  float acc=0;
  for(int n=0;n<N;n++){
    float ar=-rbf(expf(bf2f(logA[d*N+n])))*dt; float ai=bf2f(Aim[d*N+n])*dt;
    float cr=bf2f(Cr[d*N+n])*dt, ci=bf2f(Ci[d*N+n])*dt;
    float e=expf(ar*pos); float s,c; sincosf(ai*pos,&s,&c);
    acc+=e*(cr*c-ci*s);
  }
  K[d*L+l]=2.f*acc;
}
__global__ void conv(const u16* x,const u16* u,const float* K,const u16* Dk,u16* h){
  int l=blockIdx.x; int d=threadIdx.x;
  float acc=0;
  for(int j=0;j<=l;j++) acc+=K[d*L+j]*bf2f(u[(l-j)*D+d]);
  float y=rbf(acc); float t=rbf(bf2f(u[l*D+d])*bf2f(Dk[d]));
  h[l*D+d]=f2bf(bf2f(x[l*D+d])+rbf(y+t));
}
__global__ void g1(const u16* xn,const u16* wg,const u16* wu,u16* hh){
  int f=blockIdx.x*256+threadIdx.x; int l0=blockIdx.y*8;
  float g[8]={0},u[8]={0};
  for(int k=0;k<D;k++){ float a=bf2f(wg[k*F+f]), b=bf2f(wu[k*F+f]);
    for(int i=0;i<8;i++){ float xv=bf2f(xn[(l0+i)*D+k]); g[i]+=xv*a; u[i]+=xv*b; } }
  for(int i=0;i<8;i++) hh[(l0+i)*F+f]=f2bf(rbf(g[i])*rbf(u[i]));
}
__global__ void g2(const u16* hh,const u16* wd,const u16* h,u16* o){
  int d=threadIdx.x; int l0=blockIdx.x*4;
  float a[4]={0};
  for(int k=0;k<F;k++){ float w=bf2f(wd[k*D+d]); for(int i=0;i<4;i++) a[i]+=bf2f(hh[(l0+i)*F+k])*w; }
  for(int i=0;i<4;i++) o[(l0+i)*D+d]=f2bf(bf2f(h[(l0+i)*D+d])+rbf(a[i]));
}

typedef __attribute__((ext_vector_type(4))) unsigned u32x4;
typedef unsigned long long u64;
__global__ __launch_bounds__(512) void rd(const u32x4* a,const u32x4* b,const u32x4* c,int n16,int narr,u32x4* dst,int wr,u64* tim){
  u64 t0=__builtin_amdgcn_s_memrealtime();
  u32x4 acc={0,0,0,0}; u32x4 r[9];
  int per=n16/256;
  #pragma unroll
  for(int j=0;j<3;j++){ const u32x4* p=(j==0?a:(j==1?b:c));
    #pragma unroll
    for(int u=0;u<3;u++){ int i=u*512+threadIdx.x; r[j*3+u]=acc; if(j<narr && i<per) r[j*3+u]=p[blockIdx.x*per+i]; } }
  asm volatile("":: "v"(r[0]));
  u64 t1=__builtin_amdgcn_s_memrealtime();
  #pragma unroll
  for(int j=0;j<3;j++)
    #pragma unroll
    for(int u=0;u<3;u++){ int i=u*512+threadIdx.x; if(wr){ if(j<narr && i<per) dst[(size_t)j*n16+blockIdx.x*per+i]=r[j*3+u]; } else acc^=r[j*3+u]; }
  if(acc.x==0x1234567 && acc.y==0x7654321) dst[threadIdx.x]=acc;
  asm volatile("s_waitcnt vmcnt(0)":::"memory");
  u64 t2=__builtin_amdgcn_s_memrealtime();
  if(threadIdx.x==0){ tim[blockIdx.x*4]=t0; tim[blockIdx.x*4+1]=t1; tim[blockIdx.x*4+2]=t2; }
}
__global__ void spin(const u64* tim,int metric){
  if(threadIdx.x) return;
  u64 mn[3],mxs[3],mx[3],f[3];
  for(int k=0;k<3;k++){ mn[k]=~0ull; mxs[k]=0; mx[k]=0; f[k]=0; for(int b=0;b<256;b++){ const u64*p=tim+k*1024+b*4; mn[k]=min(mn[k],p[0]); mxs[k]=max(mxs[k],p[0]); mx[k]=max(mx[k],p[2]); f[k]+=p[1]-p[0]; } }
  u64 v=0;
  if(metric==1) v=(mx[0]-mn[0])*256;
  if(metric==2) v=f[0];
  if(metric==3) v=(mn[1]-mx[0])*256;
  if(metric==4) v=(mn[2]-mx[1])*256;
  if(metric==5) v=(mxs[0]-mn[0])*256;
  if(metric==6) v=(mn[1]-mn[0])*256;
  if(metric==7) v=(mx[2]-mn[0])*256;
  v = v*500/256; if(v>2000000) v=2000000;
  u64 s=__builtin_amdgcn_s_memrealtime();
  while(__builtin_amdgcn_s_memrealtime()-s < v) __builtin_amdgcn_s_sleep(8);
}
extern "C" void kernel_launch(void* const* d_in, const int* in_sizes, int n_in, void* d_out, int out_size, void* d_ws, size_t ws_size, hipStream_t stream) {
  const u16* x=(const u16*)d_in[0];
  char* ws=(char*)d_ws; u64* tim=(u64*)(ws+(100<<20)); u32x4* t1=(u32x4*)(ws+(64<<20)); u32x4* t2=(u32x4*)(ws+(84<<20)); u32x4* t3=(u32x4*)(ws+(90<<20)); const u32x4* wg=(const u32x4*)d_in[8]; const u32x4* wu=(const u32x4*)d_in[9]; const u32x4* wd=(const u32x4*)d_in[10]; const u32x4* xx=(const u32x4*)d_in[0];
  u16* u=(u16*)ws; float* K=(float*)(ws+(4<<20)); u16* h=(u16*)(ws+(12<<20)); u16* xn=(u16*)(ws+(16<<20)); u16* hh=(u16*)(ws+(20<<20));
  rd<<<256,512,0,stream>>>(xx,xx,xx,147456,1,t1,1,tim); rd<<<256,512,0,stream>>>(t1,t1,t1,147456,1,t2,1,tim+1024); rd<<<256,512,0,stream>>>(t2,t2,t2,147456,1,t3,1,tim+2048);
  spin<<<1,64,0,stream>>>(tim,0);
  ln_kernel<<<L,64,0,stream>>>(x,u);
  kgen<<<dim3(L/256,D),256,0,stream>>>((const u16*)d_in[1],(const u16*)d_in[2],(const u16*)d_in[3],(const u16*)d_in[4],(const u16*)d_in[5],K);
  conv<<<L,D,0,stream>>>(x,u,K,(const u16*)d_in[6],h);
  ln_kernel<<<L,64,0,stream>>>(h,xn);
  g1<<<dim3(F/256,L/8),256,0,stream>>>(xn,(const u16*)d_in[8],(const u16*)d_in[9],hh);
  g2<<<L/4,D,0,stream>>>(hh,(const u16*)d_in[10],h,(u16*)d_out);
}
```

```cpp
#include <hip/hip_runtime.h>
#include <stdint.h>
typedef unsigned short u16;
typedef unsigned int u32;
typedef unsigned long long u64;
typedef __attribute__((ext_vector_type(2))) u32 u32x2;
typedef __attribute__((ext_vector_type(4))) u32 u32x4;
typedef __attribute__((ext_vector_type(2))) float f32x2;
typedef __attribute__((ext_vector_type(4))) float f32x4;
typedef __attribute__((ext_vector_type(4))) short s16x4;
typedef __attribute__((ext_vector_type(2))) _Float16 f16x2;
typedef __attribute__((ext_vector_type(8))) _Float16 f16x8;
typedef __attribute__((ext_vector_type(2))) __bf16 bf16x2;
typedef __attribute__((ext_vector_type(8))) __bf16 bf16x8;
#define LDS __attribute__((address_space(3)))
#define GLB __attribute__((address_space(1)))
#define DEV __device__ __forceinline__

constexpr int SL = 2048, SD = 576, SN = 128, SF = 4608;
constexpr size_t WS_EPOCH = 0;
constexpr size_t WS_FLAG = 65536;
constexpr size_t WS_CS = 262144;
constexpr size_t WS_FX = 524288;
constexpr size_t WS_FT = WS_FX + 8 * 192 * 64;
constexpr size_t WS_HT = (size_t)1 << 20;
constexpr size_t WS_W1T = (size_t)4 << 20;
constexpr size_t WS_WDT = (size_t)15 << 20;
constexpr size_t WS_HH = (size_t)21 << 20;
constexpr size_t WS_PART = (size_t)47 << 20;
constexpr size_t WS_UX = (size_t)40 << 20;

constexpr int UXS = 32896, HTS = 4352, PTS = 8192;
constexpr size_t WS_USED = (size_t)64 << 20;
DEV float bf2f(u32 v) { return __uint_as_float(v << 16); }
DEV float bflo(u32 v) { return __uint_as_float(v << 16); }
DEV float bfhi(u32 v) { return __uint_as_float(v & 0xffff0000u); }
DEV u32 pkbf(float a, float b) { f32x2 v = {a, b}; return __builtin_bit_cast(u32, __builtin_convertvector(v, bf16x2)); }
DEV float rbf(float a) { return bflo(pkbf(a, a)); }
DEV u32 pkh(float a, float b) { f32x2 v = {a, b}; return __builtin_bit_cast(u32, __builtin_convertvector(v, f16x2)); }
template <int CTRL> DEV float dpp0(float v) {
  return __int_as_float(__builtin_amdgcn_update_dpp(0, __float_as_int(v), CTRL, 0xf, 0xf, true));
}
DEV float row_sum(float v) {
  v += dpp0<0xB1>(v); v += dpp0<0x4E>(v); v += dpp0<0x141>(v); v += dpp0<0x140>(v);
  return v;
}
DEV s16x4 trread(LDS char* p) { return __builtin_amdgcn_ds_read_tr16_b64_v4i16((LDS s16x4*)p); }
DEV u32x4 trfrag(LDS char* p0, LDS char* p1) {
  s16x4 a = trread(p0), b = trread(p1);
  return __builtin_bit_cast(u32x4, __builtin_shufflevector(a, b, 0, 1, 2, 3, 4, 5, 6, 7));
}
DEV float dot2bf(u32 a, u32 b, float c) {
  return __builtin_amdgcn_fdot2_f32_bf16(__builtin_bit_cast(bf16x2, a), __builtin_bit_cast(bf16x2, b), c, false);
}
DEV u32x4 mksrd(const void* p, u32 bytes) { return u32x4{(u32)(size_t)p, (u32)((size_t)p >> 32) & 0xffffu, bytes, 0x00020000u}; }
DEV void bst16_dev(u32x4 d, u32x4 srd, u32 voff, u32 soff) {
  asm volatile("buffer_store_dwordx4 %0, %1, %2, %3 offen sc1\n\ts_nop 1" :: "v"(d), "v"(voff), "s"(srd), "s"(soff) : "memory");
}
constexpr int G2_EARLY = 0;
constexpr unsigned CODE_LINES = 328;
constexpr int F_YPL = 2304 * 4;
constexpr int F_K = 10 * F_YPL, F_KCP = 2080 * 2, F_KCH = 4 * F_KCP;
constexpr int F_U = F_K + 3 * F_KCH, F_UR = 80 * 16 + 32, F_UCH = 4 * F_UR;
constexpr int F_SC = F_U + 3 * F_UCH;
constexpr int F_LDS = F_SC + 16;
constexpr int F_SCK = 1040, F_SCH = 32 * F_SCK, F_SCR = F_SC - 3 * F_SCH;
constexpr int F_KC = 0;
constexpr int F_ROW = 8192, F_RSTR = 328 * 4;
static_assert(F_ROW + 24 * F_RSTR <= F_SCR && F_LDS <= 163840 && F_SCR % 16 == 0, "lds map");

__host__ __device__ constexpr int g2_pos(int st, int ct) {
  return st < G2_EARLY ? st * 9 + ct : (ct == 6 || ct == 7) ? 126 + 2 * st + ct - 6 : 2 * G2_EARLY + 7 * st + (ct == 8 ? 6 : ct);
}
__host__ __device__ constexpr int ftile(int s, int i) { return 96 * (i / 3) + 3 * s + i % 3; }
struct FTab {
  u16 tap[2056];
};
constexpr FTab mk_ftab() {
  FTab t{};
  for (int e = 0; e < 2056; ++e) {
    const int l = e - 4;
    if (l < 0 || l >= 2048) { t.tap[e] = 48 * 8; continue; }
    const int sh = l < 256 ? 0 : l < 512 ? 1 : l < 1024 ? 2 : 3;
    int q = l >> sh;
    const int rem = l - (q << sh), half = (1 << sh) >> 1;
    if (sh && (rem > half || (rem == half && (q & 1)))) ++q;
    const int p = q << sh, ex = p < 256 ? 0 : p < 512 ? 1 : p < 1024 ? 2 : p < 2048 ? 3 : 4;
    const int i = (ex ? 8 + 8 * ex : 0) + ((((p >> 7) & ((1 << ex) - 1)) >> 1) << (4 - ex)) + ((p & 15) >> ex);
    t.tap[e] = (u16)((((p >> 4) & 15) * 49 + i) * 8);
  }
  return t;
}
static __device__ const __attribute__((aligned(64))) FTab ftab = mk_ftab();

#define SB __builtin_amdgcn_sched_barrier(0)
#define OPAQUE(v) asm volatile("" : "+v"(v))
#define OPAQUES(v) asm volatile("" : "+s"(v))
DEV f32x2 cmul(f32x2 z, f32x2 m) {
  f32x2 o;
  asm("v_pk_mul_f32 %0, %1, %2 op_sel_hi:[1,0]\n\tv_pk_fma_f32 %0, %1, %2, %0 op_sel:[1,1,0] op_sel_hi:[0,1,1] neg_lo:[1,0,0]"
      : "=&v"(o) : "v"(z), "v"(m));
  return o;
}
DEV void cmul2(f32x2& o0, f32x2 z0, f32x2 m0, f32x2& o1, f32x2 z1, f32x2 m1) {
  f32x2 a, b;
  asm("v_pk_mul_f32 %0, %2, %3 op_sel_hi:[1,0]\n\tv_pk_mul_f32 %1, %4, %5 op_sel_hi:[1,0]\n\t"
      "v_pk_fma_f32 %0, %2, %3, %0 op_sel:[1,1,0] op_sel_hi:[0,1,1] neg_lo:[1,0,0]\n\t"
      "v_pk_fma_f32 %1, %4, %5, %1 op_sel:[1,1,0] op_sel_hi:[0,1,1] neg_lo:[1,0,0]"
      : "=&v"(a), "=&v"(b) : "v"(z0), "v"(m0), "v"(z1), "v"(m1));
  o0 = a; o1 = b;
}
DEV f32x2 cexp_t(float ar2, float air, float t) {
  const float e = __builtin_amdgcn_exp2f(ar2 * t);
  const float ph = __builtin_amdgcn_fractf(air * t);
  return f32x2{e * __builtin_amdgcn_cosf(ph), e * __builtin_amdgcn_sinf(ph)};
}
template <int CTRL> DEV u32 dppu(u32 v) { return (u32)__builtin_amdgcn_update_dpp(0, (int)v, CTRL, 0xf, 0xf, true); }
DEV LDS char* ldsp(u32 a) { return (LDS char*)(size_t)a; }

struct ConvFr { u32x2 A[4][3]; u32x4 B[4]; };
DEV u32x2 trr(LDS char* p) { return __builtin_bit_cast(u32x2, trread(p)); }
DEV void conv_rd(ConvFr& f, LDS char* kb, LDS char* ub, int i) {
  if (i < 3) f.A[0][i] = trr(kb + i * 32);
  else if (i < 7) f.B[i - 3] = *(LDS u32x4*)(ub + (i - 3) * 256);
  else if (i < 16) f.A[1 + (i - 7) / 3][(i - 7) % 3] = trr(kb + (1 + (i - 7) / 3) * 1024 + ((i - 7) % 3) * 32);
}
DEV void conv_load(ConvFr& f, LDS char* kb, LDS char* ub) {
#pragma unroll
  for (int i = 0; i < 16; ++i) conv_rd(f, kb, ub, i);
}
#define MH(c, a, b) asm volatile("v_mfma_f32_16x16x32_f16 %0, %1, %2, %0" : "+v"(c) : "v"(a), "v"(b))
template <bool LAST, class F> DEV void conv_unit(f32x4 (&acc)[4][2], const ConvFr& f, F side) {
  u32x4 a0[4], a1[4];
  int n = 0;
#pragma unroll
  for (int o = 0; o < 4; ++o) {
    a0[o] = u32x4{f.A[o][0][0], f.A[o][0][1], f.A[o][1][0], f.A[o][1][1]};
    a1[o] = u32x4{f.A[o][1][0], f.A[o][1][1], f.A[o][2][0], f.A[o][2][1]};
    asm volatile("s_nop 0" : "+v"(a0[o]), "+v"(a1[o]));
#pragma unroll
    for (int i = 0; i < (LAST ? o + 1 : 4 - o); ++i) {
      const int a = LAST ? i : o, T = LAST ? o : o + i;
      MH(acc[T][0], a0[a], f.B[T - a]); side(n++); SB;
      MH(acc[T][1], a1[a], f.B[T - a]); side(n++); SB;
    }
  }
}
DEV void conv_flush(f32x4 (&acc)[4][2], LDS char* yb) {
  asm volatile("s_nop 7" ::: "memory");
#pragma unroll
  for (int T = 0; T < 4; ++T)
#pragma unroll
    for (int t = 0; t < 2; ++t) {
      *(LDS f32x4*)(yb + T * (16 * 144) + t * 64) = acc[T][t];
      acc[T][t] = f32x4{0.f, 0.f, 0.f, 0.f};
      OPAQUE(acc[T][t]);
    }
  asm volatile("s_nop 1" ::: "memory");
}
#define CPR(n) __builtin_amdgcn_s_setprio(n)

struct FrontOut { u32 tag, word; };
DEV FrontOut front_phase(const u16* x, const u16* logA, const u16* Aim, const u16* Cre, const u16* Cim, const u16* logdt, const u16* Dsk,
                    const u16* wg, const u16* wu, const u16* wd, char* ws) {
  extern __shared__ __attribute__((aligned(16))) char smem_[];
  LDS char* sm = (LDS char*)smem_;
  const u32 sm0 = (u32)(size_t)sm;
  const int tid = threadIdx.x, lane = tid & 63, w = __builtin_amdgcn_readfirstlane(tid >> 6);
  const int bid = blockIdx.x, xcd = bid & 7, idx = bid >> 3;
  const int G = lane >> 4, qq = (lane >> 2) & 3, p = lane & 3, rr = lane >> 3;
  GLB u32* epoch = (GLB u32*)(ws + WS_EPOCH);
  u32 cpf;
  u64 pc0;
  asm volatile("s_getpc_b64 %0" : "=s"(pc0));
  pc0 &= ~(u64)127;
  cpf = ((const GLB u32*)pc0)[(w * 16 + (lane & 15)) * 32];
  if (idx >= 24) {
    u32 tag0 = __hip_atomic_load(epoch, __ATOMIC_RELAXED, __HIP_MEMORY_SCOPE_AGENT);
    const int sp = xcd * 8 + idx - 24, task = sp * 7 + w;
    const bool act = w < 7 && task < 432;
    const int fb = task / 144 * 24 + task % 24, mat = (task / 24) & 1, kt = task % 144 / 48;
    u32x4 wv[6][4];
    f32x4 cs = {0.f, 0.f, 0.f, 0.f};
    if (act) {
      const u16* src = (mat ? wu : wg) + (size_t)(kt * 192 + rr) * SF + fb * 64 + (lane & 7) * 8;
#pragma unroll
      for (int i = 0; i < 6; ++i)
#pragma unroll
        for (int e = 0; e < 4; ++e) wv[i][e] = __builtin_nontemporal_load((const u32x4*)(src + (size_t)(i * 32 + e * 8) * SF));
      LDS char* tb = sm + w * 8192;
      const u32 atr = (8 * G + qq) * 128 + ((((qq >> 1) & 1) | ((G & 1) << 1)) << 5) + p * 8;
#pragma unroll
      for (int i = 0; i < 7; ++i) {
        SB;
        if (i < 6) {
#pragma unroll
          for (int e = 0; e < 4; ++e)
            *(LDS u32x4*)(tb + (i & 1) * 4096 + (e * 8 + rr) * 128 + (((lane & 7) ^ ((((rr >> 1) & 1) | ((e & 1) << 1)) << 1)) << 4)) = wv[i][e];
#pragma unroll
          for (int f = 0; f < 4; ++f) wv[i][f] = trfrag(tb + (i & 1) * 4096 + (atr ^ (f << 5)), tb + (i & 1) * 4096 + (atr ^ (f << 5)) + 512);
        }
        if (i) {
#pragma unroll
          for (int f = 0; f < 4; ++f) {
            const u32 o1 = (lane & 15) == f ? 0x3f803f80u : 0u;
            cs = __builtin_amdgcn_mfma_f32_16x16x32_bf16(__builtin_bit_cast(bf16x8, wv[i - 1][f]), __builtin_bit_cast(bf16x8, u32x4{o1, o1, o1, o1}), cs, 0, 0, 0);
          }
        }
      }
      SB;
    }
    if (w == 7) {
      cpf ^= ((const GLB u32*)pc0)[min(128 + (idx - 24) * 26 + min(lane, 25), (int)CODE_LINES - 1) * 32];
    }
    asm volatile("" : "+v"(tag0) : "v"(cpf));
    asm volatile("s_barrier" ::: "memory");
    const auto rws = __builtin_amdgcn_make_buffer_rsrc(ws, 0, WS_HT, 0x00020000);
    if (act) {
      const auto rw1 = __builtin_amdgcn_make_buffer_rsrc(ws + WS_W1T, 0, 2 * SF * SD * 2, 0x00020000);
#pragma unroll
      for (int f = 0; f < 4; ++f) {
        const int T = 4 * (task % 24) + f;
        const u32 dst = (((T / 3 * 3 + task / 144) * 18 + kt * 6) * 6 + 2 * (T % 3) + mat) * 1024 + lane * 16;
#pragma unroll
        for (int i = 0; i < 6; ++i) __builtin_amdgcn_raw_buffer_store_b128(wv[i][f], rw1, dst + i * 6144, 0, 16);
      }
      if ((lane & 15) < 4)
        __builtin_amdgcn_raw_buffer_store_b128(__builtin_bit_cast(u32x4, cs), rws,
                                               WS_CS + ((kt * 6 + task / 144 * 2 + mat) * 96 + 4 * (task % 24) + (lane & 15)) * 64 + G * 16, 0, 16);
    }
    return FrontOut{tag0 + 1u, act ? (u32)WS_FT + task * 64 : 1u << 30};
  }

  const int g = xcd * 24 + idx, ch0 = 3 * g;
  const int hg = w * 192 + g;
  const bool lnw = hg < 512;
  u32x4 rx[5];
  if (lnw) {
    const auto rsxin = __builtin_amdgcn_make_buffer_rsrc((void*)x, 0, SL * SD * 2, 0x00020000);
    const u32 so = (16 * (hg >> 2) + (hg & 3)) * (SD * 2), vo = G * (4 * SD * 2) + (lane & 15) * 16;
#pragma unroll
    for (int k = 0; k < 4; ++k) rx[k] = __builtin_amdgcn_raw_buffer_load_b128(rsxin, vo + k * 256, so, 2);
    rx[4] = __builtin_amdgcn_raw_buffer_load_b128(rsxin, (lane & 8) ? SL * SD * 2 : vo + 1024, so, 2);
  }
  u32 tag = __hip_atomic_load(epoch, __ATOMIC_RELAXED, __HIP_MEMORY_SCOPE_AGENT);
  const int cA = w < 4 ? 2 : 0, cB = w < 4 ? 2 : 1;
  u32 pA[2], pI[2], pR[2], pM[2];
  {
    auto ld = [&](const u16* base, int c) { return *(const u32*)((const char*)(base + c * SN) + (u32)lane * 4); };
#pragma unroll
    for (int k = 0; k < 2; ++k) {
      const int c = ch0 + (k ? cB : cA);
      pA[k] = ld(logA, c); pI[k] = ld(Aim, c); pR[k] = ld(Cre, c); pM[k] = ld(Cim, c);
    }
  }
  struct U2 { u32 a, b; };
  typedef const __attribute__((address_space(4))) u32* cptr;
  const cptr pdt = (cptr)(size_t)(logdt + ch0 - (g & 1)), pds = (cptr)(size_t)(Dsk + ch0 - (g & 1));
  const U2 sdt = {pdt[0], pdt[1]}, sds = {pds[0], pds[1]};
  auto pick = [&](U2 v, int c) { return bf2f((u32)(((((u64)v.b << 32) | v.a) >> (16 * (c + (g & 1)))) & 0xffffu)); };
  struct __attribute__((packed, aligned(8))) T16 { u32x2 a, b; };
  const T16 tp = *(const T16*)((const char*)ftab.tap + (u32)tid * 8);
  asm volatile("" ::: "memory");
  SB;
  u32 akb[3], aub[3];
#pragma unroll
  for (int j = 0; j < 3; ++j) {
    const int u = 6 * w + 2 * j;
    akb[j] = sm0 + F_K + (4 + 4 * p + 4 * (8 * G + qq)) * 2 + (u >> 2) * F_KCP + (u & 3) * 256;
    aub[j] = sm0 + F_U + (16 + (lane & 15) - G) * 16 + (u >> 2) * F_UR - (u & 3) * 64 - 64;
    OPAQUE(akb[j]); OPAQUE(aub[j]);
  }
  u32 akw = sm0 + F_K + 64 + tid * 8, auw = sm0 + F_U + p * F_UR + (16 + (tid >> 3)) * 16 + (((tid >> 2) & 1) ^ 1) * 8;
  u32 ayl = sm0 + (36 * (lane & 15) + 4 * G) * 4, ayr = sm0 + (36 * (tid >> 3) + 4 * (tid & 7)) * 4, ayr2 = ayr + 7 * F_YPL;
  OPAQUE(akw); OPAQUE(auw); OPAQUE(ayl); OPAQUE(ayr); OPAQUE(ayr2);
  if (tid == 0) *(LDS u32x2*)(sm + F_KC + 48 * 8) = u32x2{0, 0};
  SB;
  OPAQUE(tag);
  tag += 1u;
  const u32 tagp = (tag & 0xffffu) | ((tag ^ (tag << 16)) & 0xffff0000u);
  asm volatile("" : "+v"(pA[0]), "+v"(pI[0]), "+v"(pR[0]), "+v"(pM[0]), "+v"(pA[1]), "+v"(pI[1]), "+v"(pR[1]), "+v"(pM[1]) : "v"(cpf));
  u32 tpa[4] = {tp.a[0], tp.a[1], tp.b[0], tp.b[1]};
  asm volatile("" : "+v"(tpa[0]), "+v"(tpa[1]), "+v"(tpa[2]), "+v"(tpa[3]));
  auto publish = [&](int set, int isx) {
    const u32x4 rsx = mksrd(ws + WS_UX + (size_t)isx * 96 * UXS, 96 * UXS);
    u32 ard = sm0 + F_ROW + (set * 8 + isx * 4 + p) * F_RSTR + (lane >> 2) * 12;
    OPAQUE(ard);
    const int h = set * 192 + g;
    const u32 vo = (lane >> 2) * UXS + p * 16, so = (h & 3) * 8192 + (h >> 2) * 64;
    u32 d[6][3];
#pragma unroll
    for (int k = 0; k < 6; ++k)
#pragma unroll
      for (int i = 0; i < 3; ++i) d[k][i] = ((LDS u32*)ldsp(ard))[k * 48 + i];
    SB;
#pragma unroll
    for (int k = 0; k < 6; ++k)
      bst16_dev(u32x4{d[k][0], __builtin_amdgcn_perm(tagp, d[k][1], 0x05040100u), __builtin_amdgcn_perm(d[k][2], d[k][1], 0x05040302u),
                      __builtin_amdgcn_perm(tagp, d[k][2], 0x07060302u)}, rsx, vo, so + k * (16 * UXS));
  };
  if (lnw) {
    __builtin_amdgcn_s_setprio(2);
    LDS char* rw = sm + F_ROW + w * (8 * F_RSTR);
    LDS char* wr = rw + G * F_RSTR + (lane & 15) * 16;
    f32x2 v[20];
#pragma unroll
    for (int i = 0; i < 20; ++i) v[i] = f32x2{bflo(rx[i >> 2][i & 3]), bfhi(rx[i >> 2][i & 3])};
    float s0 = 0.f, s1 = 0.f, q0 = 0.f, q1 = 0.f;
#pragma unroll
    for (int i = 0; i < 20; i += 2) {
      const u32 a = rx[i >> 2][i & 3], b = rx[i >> 2][(i & 3) + 1];
      s0 = dot2bf(a, 0x3f803f80u, s0); q0 = dot2bf(a, a, q0); s1 = dot2bf(b, 0x3f803f80u, s1); q1 = dot2bf(b, b, q1);
    }
    const float mean = row_sum(s0 + s1) * (1.f / SD);
    const float var = rbf(fmaxf(row_sum(q0 + q1) * (1.f / SD) - mean * mean, 0.f));
    const float rs = __builtin_amdgcn_rcpf(rbf(__builtin_amdgcn_sqrtf(rbf(var + 1e-5f)))), nb = -rbf(mean) * rs;
#pragma unroll
    for (int k = 0; k < 5; ++k) {
      u32x4 o;
#pragma unroll
      for (int i = 0; i < 4; ++i) { const f32x2 u = v[4 * k + i] * rs + nb; o[i] = pkh(u[0], u[1]); }
      *(LDS u32x4*)(wr + k * 256) = o;
    }
    publish(w, 0);
#pragma unroll
    for (int k = 0; k < 5; ++k) *(LDS u32x4*)(wr + 4 * F_RSTR + k * 256) = rx[k];
    __builtin_amdgcn_s_setprio(0);
  }

#pragma clang loop unroll(disable)
  for (int c = cA; c <= cB; ++c) {
    constexpr int k = 0;
    const u32 hs = (~w & 2) << 3;
    auto half = [&](u32 v) { return bfhi(v << hs); };
    const float dt = rbf(__expf(pick(sdt, c)));
    const float ar = -rbf(__expf(half(pA[k]))) * dt * 1.44269504088896341f, ai = half(pI[k]) * dt * 0.15915494309189535f;
    u32 atb = sm0 + F_SCR + c * F_SCH + (((w >> 1) & 1) * 16 + (lane >> 2)) * F_SCK + p * 4;
    OPAQUE(atb);
    auto put = [&](int r0, f32x2 z0, int r1, f32x2 z1) {
      asm volatile("ds_write2_b32 %0, %1, %2 offset0:%3 offset1:%4" :: "v"(atb), "v"(pkh(z0[0], z0[1])), "v"(pkh(z1[0], z1[1])), "i"(r0 * 4), "i"(r1 * 4) : "memory");
    };
    const f32x2 p256 = cexp_t(ar, ai, 256.f), pb = cexp_t(ar, ai, (w & 1) ? 8.f : 1.f);
    f32x2 p512, pb2;
    cmul2(p512, p256, p256, pb2, pb, pb);
    if (!(w & 1)) {
      f32x2 ze = pb2, zo, p768, zn, t0, tp0 = p256;
      cmul2(p768, p512, p256, zo, pb, pb2);
      put(0, f32x2{1.f, 0.f}, 1, pb); put(24, p512, 28, p768);
#pragma unroll
      for (int i = 1; i < 8; ++i) {
        cmul2(t0, ze, p256, zn, ze, pb2);
        put(2 * i, ze, 2 * i + 1, zo);
        if (i < 7) zo = cmul(zo, pb2);
        if (!(i & 1)) { f32x2 t1, t2; cmul2(t1, ze, p512, t2, ze, p768); put(24 + i / 2, t1, 28 + i / 2, t2); tp0 = t0; }
        else put(15 + i, tp0, 16 + i, t0);
        ze = zn;
      }
    } else {
      u32 mx = max(max(pR[k] & 0x7fffu, pM[k] & 0x7fffu), max(pR[k] & 0x7fffffffu, pM[k] & 0x7fffffffu) >> 16);
      mx = max(mx, dppu<0xB1>(mx)); mx = max(mx, dppu<0x4E>(mx)); mx = max(mx, dppu<0x141>(mx)); mx = max(mx, dppu<0x140>(mx));
      u32 m01 = max((u32)__builtin_amdgcn_readlane(mx, 0), (u32)__builtin_amdgcn_readlane(mx, 16));
      u32 m23 = max((u32)__builtin_amdgcn_readlane(mx, 32), (u32)__builtin_amdgcn_readlane(mx, 48));
      OPAQUES(m01); OPAQUES(m23);
      u32 eb = max((max(m01, m23) >> 7) + 1u, 2u);
      OPAQUES(eb);
      eb = min(eb, 252u);
      const float inv = __uint_as_float((254u - eb) << 23);
      *(LDS float*)(sm + F_SC + 4 * c) = 2.f * dt * __uint_as_float(eb << 23);
      const f32x2 q16 = {pb2[0], -pb2[1]};
      f32x2 ye = {half(pR[k]) * inv, -half(pM[k]) * inv}, yo, z, q32, t0;
      cmul2(q32, q16, q16, z, p512, p512);
      yo = cmul(ye, q16);
#pragma unroll
      for (int i = 0; i < 8; ++i) {
        put(48 + 2 * i, ye, 49 + 2 * i, yo);
        if (i < 7) cmul2(ye, ye, q32, yo, yo, q32);
        if (i < 4) { f32x2 zn; cmul2(t0, z, pb, zn, z, p256); put(32 + 2 * i, z, 33 + 2 * i, t0); z = zn; }
        if (i == 4) put(40, z, 41, z);
      }
    }
    pA[0] = pA[1]; pI[0] = pI[1]; pR[0] = pR[1]; pM[0] = pM[1];
  }
  if (lnw) publish(w, 1);
  asm volatile("s_waitcnt lgkmcnt(0)" ::: "memory");
  __syncthreads();
  auto un_load = [&](u32x4 (&un)[4], int isx) {
    const u32x4 rux = mksrd(ws + WS_UX + (size_t)(isx * 96 + (g >> 1)) * UXS, 32768);
    asm volatile("buffer_load_dwordx4 %0, %4, %5, 0 offen sc1\n\tbuffer_load_dwordx4 %1, %4, %5, %6 offen sc1\n\t"
                 "buffer_load_dwordx4 %2, %4, %5, %7 offen sc1\n\tbuffer_load_dwordx4 %3, %4, %5, %8 offen sc1"
                 : "=&v"(un[0]), "=&v"(un[1]), "=&v"(un[2]), "=&v"(un[3]) : "v"(tid * 16), "s"(rux), "s"(8192), "s"(16384), "s"(24576) : "memory");
  };
  auto un_wait = [&](u32x4 (&un)[4], int isx, u32x2 (&o)[4]) {
    auto stale = [&]() {
      u32 bad = 0;
#pragma unroll
      for (int e = 0; e < 4; ++e) bad |= __builtin_amdgcn_perm(un[e][3], un[e][1], 0x07060302u) ^ tagp;
      return __any(bad != 0);
    };
    if (__builtin_expect(stale(), 0)) {
      u32 spin = 0;
#pragma clang loop unroll(disable)
      do {
        __builtin_amdgcn_s_sleep(2);
        un_load(un, isx);
        asm volatile("s_waitcnt vmcnt(0)" : "+v"(un[0]), "+v"(un[1]), "+v"(un[2]), "+v"(un[3]) :: "memory");
      } while (stale() && ++spin < (1u << 21));
    }
#pragma unroll
    for (int e = 0; e < 4; ++e) o[e] = (g & 1) ? u32x2{un[e][2], un[e][3]} : u32x2{un[e][0], un[e][1]};
  };
  u32x4 un[4];
  u32x2 uu[4], xx[4];
  u32 pf3;
  auto wd_pf = [&]() {
    asm volatile("global_load_dword %0, %1, %2" : "=v"(pf3) : "v"(((g * 8 + w) * 27 + min(lane, 26)) * 128), "s"(wd) : "memory");
  };
  un_load(un, 0);
  if (w < 3) {
    u32 ab = sm0 + F_SCR + w * F_SCH + (lane & 15) * 16 + ((G & 1) * 16 + (G >> 1) * 8) * F_SCK;
    OPAQUE(ab);
    f16x8 fr[8][4];
#pragma unroll
    for (int ks = 0; ks < 8; ++ks)
#pragma unroll
      for (int r = 3; r >= 0; --r) fr[ks][r] = *(LDS f16x8*)(ldsp(ab) + r * 256 + ks * F_SCK);
    f32x4 kc[3] = {{0, 0, 0, 0}, {0, 0, 0, 0}, {0, 0, 0, 0}};
#pragma unroll
    for (int ks = 0; ks < 8; ++ks)
#pragma unroll
      for (int r = 2; r >= 0; --r) kc[r] = __builtin_amdgcn_mfma_f32_16x16x32_f16(fr[ks][r], fr[ks][3], kc[r], 0, 0, 0);
    __builtin_amdgcn_sched_group_barrier(0x100, 12, 0);
#pragma unroll
    for (int i = 0; i < 20; ++i) {
      __builtin_amdgcn_sched_group_barrier(0x8, 1, 0);
      __builtin_amdgcn_sched_group_barrier(0x100, 1, 0);
    }
    SB;
    LDS char* o = sm + F_KC + ((lane & 15) * 49 + G * 4) * 8 + w * 2;
#pragma unroll
    for (int r = 2; r >= 0; --r) {
      const u32 h0 = pkh(kc[r][0], kc[r][1]), h1 = pkh(kc[r][2], kc[r][3]);
      *(LDS u16*)(o + r * 128) = (u16)h0; *(LDS u16*)(o + r * 128 + 8) = (u16)(h0 >> 16);
      *(LDS u16*)(o + r * 128 + 16) = (u16)h1; *(LDS u16*)(o + r * 128 + 24) = (u16)(h1 >> 16);
    }
  }
  __syncthreads();
  {
    u32x2 k[7];
#pragma unroll
    for (int e = 0; e < 7; ++e) {
      const u32 d = tpa[(e + 1) >> 1];
      k[e] = *(LDS u32x2*)(sm + F_KC + ((e & 1) ? d & 0xffffu : d >> 16));
    }
#pragma unroll
    for (int c = 0; c < 3; ++c) {
      u32 pr[6];
#pragma unroll
      for (int e = 0; e < 6; ++e) pr[e] = __builtin_amdgcn_perm(k[e + 1][c >> 1], k[e][c >> 1], (c & 1) ? 0x07060302u : 0x05040100u);
#pragma unroll
      for (int r = 0; r < 4; ++r) *(LDS u32x2*)(ldsp(akw) + c * F_KCH + r * F_KCP) = u32x2{pr[3 - r], pr[5 - r]};
    }
  }
  if (tid < 240) *(LDS u32x4*)(sm + (tid < 192 ? F_U + (tid >> 4) * F_UR + (tid & 15) * 16 : F_K + ((tid - 192) >> 2) * F_KCP + (tid & 3) * 16)) = u32x4{0, 0, 0, 0};
  {
    asm volatile("s_waitcnt vmcnt(0)" : "+v"(un[0]), "+v"(un[1]), "+v"(un[2]), "+v"(un[3]) :: "memory");
    un_wait(un, 0, uu);

    const bool b1 = lane & 2;
    const u32 sel = (lane & 1) ? 0x03020706u : 0x05040100u;
#pragma unroll
    for (int c = 0; c < 3; ++c) {
      const u32 ps = (c & 1) ? 0x07060302u : 0x05040100u;
      const u32 a0 = __builtin_amdgcn_perm(uu[1][c >> 1], uu[0][c >> 1], ps), a1 = __builtin_amdgcn_perm(uu[3][c >> 1], uu[2][c >> 1], ps);
      const u32 rv = dppu<0x4E>(b1 ? a0 : a1);
      const u32 lo = b1 ? rv : a0, hi = b1 ? a1 : rv;
      *(LDS u32x2*)(ldsp(auw) + c * F_UCH) = u32x2{__builtin_amdgcn_perm(hi, dppu<0xB1>(hi), sel), __builtin_amdgcn_perm(lo, dppu<0xB1>(lo), sel)};
    }
  }
  __syncthreads();
  f32x4 base[3];
  const float dsk[3] = {pick(sds, 0), pick(sds, 1), pick(sds, 2)};
  {
    f32x4 acc[4][2];
    ConvFr fa, fb;
    u32 bad = 0;
    conv_load(fa, ldsp(akb[0]), ldsp(aub[0]) + 64);
#pragma unroll
    for (int i = 0; i < 8; ++i) { acc[i >> 1][i & 1] = f32x4{0.f, 0.f, 0.f, 0.f}; OPAQUE(acc[i >> 1][i & 1]); }
    SB;
    CPR(3);
    conv_unit<false>(acc, fa, [&](int i) {
      conv_rd(fb, ldsp(akb[0]) + 256, ldsp(aub[0]), i);
      if (i == 0) un_load(un, 1);
      if (i == 12) wd_pf();
    });
    conv_unit<false>(acc, fb, [&](int i) {
      conv_rd(fa, ldsp(akb[1]), ldsp(aub[1]) + 64, i);
    });
    if (w == 5) conv_flush(acc, ldsp(ayl) + 6 * F_YPL);
    CPR(2);
    conv_unit<false>(acc, fa, [&](int i) {
      conv_rd(fb, ldsp(akb[1]) + 256, ldsp(aub[1]), i);
    });
    conv_unit<false>(acc, fb, [&](int i) {
      conv_rd(fa, ldsp(akb[2]), ldsp(aub[2]) + 64, i);
      if (i == 8) asm volatile("s_waitcnt vmcnt(1)" : "+v"(un[0]), "+v"(un[1]), "+v"(un[2]), "+v"(un[3]) :: "memory");
      if (i >= 9 && i < 17) {
        const int e = (i - 9) >> 1;
        if (i & 1) { bad |= __builtin_amdgcn_perm(un[e][3], un[e][1], 0x07060302u) ^ tagp; OPAQUE(bad); }
        else { xx[e] = (g & 1) ? u32x2{un[e][2], un[e][3]} : u32x2{un[e][0], un[e][1]}; OPAQUE(xx[e]); }
      }
    });
    if (w == 2) conv_flush(acc, ldsp(ayl) + 2 * F_YPL);
    if (__builtin_expect(__any(bad != 0), 0)) un_wait(un, 1, xx);
    CPR(1);
    conv_unit<false>(acc, fa, [&](int i) {
      conv_rd(fb, ldsp(akb[2]) + 256, ldsp(aub[2]), i);
      if (i < 12) {
        const int c = i >> 2, e = i & 3;
        const u32 uw = uu[e][c >> 1], xw = xx[e][c >> 1];
        float xf = (c & 1) ? bfhi(xw) : bflo(xw);
        if (c & 1) asm volatile("v_fma_mix_f32 %0, %1, %2, %0 op_sel:[1,0,0] op_sel_hi:[1,0,0]" : "+v"(xf) : "v"(uw), "s"(dsk[c]));
        else asm volatile("v_fma_mix_f32 %0, %1, %2, %0 op_sel_hi:[1,0,0]" : "+v"(xf) : "v"(uw), "s"(dsk[c]));
        base[c][e] = xf;
      }
    });
    CPR(0);
    LDS char* yb = ldsp(ayl) + (w + ((6 * w + 5) >> 4)) * F_YPL;
    conv_unit<true>(acc, fb, [&](int i) {
      if (i == 4 || i == 5) *(LDS f32x4*)(yb + (i - 4) * 64) = acc[0][i - 4];
      if (i == 8 || i == 9) *(LDS f32x4*)(yb + 16 * 144 + (i - 8) * 64) = acc[1][i - 8];
      if (i == 14 || i == 15) *(LDS f32x4*)(yb + 32 * 144 + (i - 14) * 64) = acc[2][i - 14];
    });
    asm volatile("s_nop 7" : "+v"(acc[3][0]), "+v"(acc[3][1]));
    *(LDS f32x4*)(yb + 48 * 144) = acc[3][0]; *(LDS f32x4*)(yb + 48 * 144 + 64) = acc[3][1];
  }
  __syncthreads();
  {
    const u32x4 rh = mksrd(ws + WS_HT + (size_t)ch0 * HTS, 3 * HTS);
    u32x2 hb[3];
#pragma unroll
    for (int c = 0; c < 3; ++c) {
      LDS char* yr = c < 2 ? ldsp(ayr) + 3 * c * F_YPL : ldsp(ayr2);
      f32x4 y = (*(LDS f32x4*)yr + *(LDS f32x4*)(yr + F_YPL)) + *(LDS f32x4*)(yr + 2 * F_YPL);
      if (c == 1) y += *(LDS f32x4*)(yr + 3 * F_YPL);
      const f32x4 h = y * *(LDS float*)(sm + F_SC + 4 * c) + base[c];
      hb[c] = u32x2{pkbf(h[0], h[1]), pkbf(h[2], h[3])};
    }
    asm volatile("s_waitcnt vmcnt(0)" :: "v"(pf3) : "memory");
    const bool od = lane & 1;
    const u32 s0 = dppu<0xB1>(od ? hb[0][0] : hb[1][0]), s1 = dppu<0xB1>(od ? hb[0][1] : hb[1][1]);
    const u32 vo = (tid >> 1) * 16 + (od ? HTS : 0);
    bst16_dev(od ? u32x4{s0, s1, hb[1][0], hb[1][1]} : u32x4{hb[0][0], hb[0][1], s0, s1}, rh, vo, 0);
    bst16_dev(u32x4{hb[2][0], hb[2][1], dppu<0xB1>(hb[2][0]), dppu<0xB1>(hb[2][1])}, rh, vo, 2 * HTS);
  }

  return FrontOut{tag, (u32)WS_FX + (w * 192 + g) * 64};
}
constexpr int M_CS = 19 * 8192, M_RS = M_CS + 2048, M_LDS = M_RS + 768;
constexpr u32 FH_OFF = WS_FLAG;
DEV u32 fresh(u32 v) { OPAQUE(v); return v; }
DEV u32x4 pk8(f32x4 a, f32x4 b) { return u32x4{pkbf(a[0], a[1]), pkbf(a[2], a[3]), pkbf(b[0], b[1]), pkbf(b[2], b[3])}; }

DEV void mlp_phase(char* ws, const u16* wd, u16* out, FrontOut fo) {
  const u32 tag = __builtin_amdgcn_readfirstlane(fo.tag);
  extern __shared__ __attribute__((aligned(16))) char smem_[];
  const u32 sm0 = (u32)(size_t)(LDS char*)smem_;
  int tid = threadIdx.x;
  OPAQUE(tid);
  const int lane = tid & 63, w = __builtin_amdgcn_readfirstlane(tid >> 6);
  const int bid = blockIdx.x, kap = bid & 3, pan = 4 * ((bid >> 2) & 1) + (bid >> 6), sl = (bid >> 3) & 7, s = 8 * kap + sl;
  const int rho = sl >> 2, cc = sl & 3, rg = w & 3, kg = w >> 2, G = lane >> 4;
  const u32x4 srd = mksrd(ws, WS_USED);
  const auto rsw = __builtin_amdgcn_make_buffer_rsrc(ws, 0, WS_USED, 0x00020000);
  const u32 sL = sm0 + w * 1024;
  const u32 sX = WS_HT + pan * 512;
  const u32 sW1 = WS_W1T + (s * 324 + w) * 1024, sWd = WS_WDT + ((kap * 4 + cc) * 324 + w - 4) * 1024, sWm = w < 4 ? sW1 + 40 * 8192 : sWd;
  u32 vL16 = lane * 16, vXs;
  OPAQUE(vL16);
  {
    const int lr = 2 * w + (lane >> 5);
    vXs = (8 * (lr >> 2) + (lr & 3)) * HTS + (((lane & 31) ^ ((lr & 7) << 1)) << 4);
  }
#define DMA(sb, si, vo, li) do { u32 t_; asm volatile("s_add_u32 m0, %1, %2\n\ts_add_u32 %0, %3, %4\n\tbuffer_load_dwordx4 %5, %6, %0 offen sc1 lds" \
    : "=&s"(t_) : "s"(sL), "i"(li), "s"(sb), "i"(si), "v"(vo), "s"(srd) : "memory", "scc"); } while (0)
  const u32 T4 = tag << 2;
  const u32 vFt = (48 * (min(w, 5) >> 1) + 24 * (min(w, 5) & 1) + ((3 * s + 2 * (lane & 1)) >> 2)) * 64;
  const int wgi = ((pan & 3) == 3 ? 6 + (pan >> 2) : pan - (pan >> 2)) * 8 + sl;
  u32 wdd[4];
  auto wt_setup = [&]() {
    const u32 last = (wgi + 28) >> 6;
    u32 k2 = wgi + last * (7 * wgi - 288 + w);
    OPAQUES(k2);
    const u32 sq = k2 & 3, j = (k2 >> 2) & 3, sl2 = 2 * sq + ((k2 >> 4) & 1), sh = k2 >> 5, off = ((k2 + 220) >> 8) << 27;
    const u32 lut = (u32)(0xC1C0626160020100ull >> (16 * j)), s0 = 3 * (8 * kap + sl2), col = (w + last * (8 - w)) * 128 + off;
    u32 st0 = 4 * j + (sl2 & 3), st1 = 16 + (sq & 1), jp = ((w >> 1) - kap - 1) & 3;
    OPAQUES(st0); OPAQUES(st1);
    const u32 st = sh ? st1 : st0;
    u32 pa = g2_pos(st, 0) + 2 * jp + last * (6 - 2 * jp), pb = g2_pos(st, 6);
    OPAQUES(pa); OPAQUES(pb);
    wdd[0] = (s0 + (lut & 255) + 194 * sh) * (32 * SD) + col;
    wdd[1] = (s0 + ((lut >> 8) & 255) + 196 * sh) * (32 * SD) + col;
    wdd[2] = WS_WDT + (kap * 1296 + (sq >> 1) + (w & 1 & ~last) * 648 + 2 * (jp - last == 3 ? pb : pa)) * 1024 + off;
    wdd[3] = last;
#pragma unroll
    for (int i = 0; i < 4; ++i) OPAQUES(wdd[i]);
  };
  static_assert(G2_EARLY == 0 && ftile(0, 3) == 0x60 && ftile(0, 6) == 0xC0 && ftile(0, 8) == 194 && ftile(1, 8) == 197, "lut");
  u32 vP;
  {
    u32 sFx = WS_FX + (pan * 8 + w) * (24 * 64), va = WS_FT + vFt;
    OPAQUES(sFx); OPAQUE(va);
    u32 vb = sFx + lane * 64;
    OPAQUE(vb);
    u32 vf = lane < 16 ? lane * 4 : 1 << 30, vp = lane < 24 ? vb : va;
    asm volatile("s_waitcnt vmcnt(0)" : "+v"(vf), "+v"(vp) :: "memory");
    __builtin_amdgcn_s_waitcnt(0x0F70);
    __builtin_amdgcn_raw_buffer_store_b32(T4, rsw, vf, __builtin_amdgcn_readfirstlane(fo.word), 16);
    asm volatile("" ::: "memory");
    SB;
    wt_setup();
    vP = vp;
  }
  const u32x4 srdw = mksrd(wd, SF * SD * 2);
  const u32 sL4 = sm0 + w * 4096;
  u32 vT;
#define WT_SRC() do { const u32 l_ = fresh(vL16) >> 4; vT = (l_ >> 3) * (SD * 2) + (((l_ & 7) ^ ((l_ >> 3) & 6)) << 4); OPAQUE(vT); } while (0)
#define WT_LD(e) do { u32 t_; asm volatile("s_add_u32 m0, %1, %2\n\ts_add_u32 %0, %3, %4\n\tbuffer_load_dwordx4 %5, %6, %0 offen lds" \
    : "=&s"(t_) : "s"(sL4), "i"((e) * 1024), "s"(wdd[(e) >> 1]), "i"(((e) & 1) * 8 * SD * 2), "v"(vT), "s"(srdw) : "memory", "scc"); } while (0)
#define WT_ADR(n) do { asm volatile("s_waitcnt vmcnt(" #n ")" ::: "memory"); const u32 l_ = fresh(vL16) >> 4, p_ = l_ & 3; \
    vT = sL4 + (l_ >> 2) * 128 + (((p_ >> wdd[3]) ^ ((l_ >> 2) & 6)) << 4) + (p_ & wdd[3]) * 8; OPAQUE(vT); } while (0)
#define WT_RD(f) trfrag(ldsp(vT ^ (((f) & 1 ? 8u << (2 * wdd[3]) : 0) | ((f) & 2 ? 64 : 0))), ldsp(vT ^ (((f) & 1 ? 8u << (2 * wdd[3]) : 0) | ((f) & 2 ? 64 : 0))) + 2048)
#define WT_ST(v, f) bst16_dev(v, srd, vL16, wdd[2] + (((f) >> 1) * 324 + ((f) & 1) * 2) * 1024 + wdd[3] * ((((f) >> 1) * 324 + ((f) & 1) * 322) * 1024))
  const u32 sWw = w == 0 ? FH_OFF + 65536 + kap * 1024 + wgi * 16 : 0x8000000u, sWr = FH_OFF + 65536 + kap * 1024 + w * 96;
#define SYNC(n) do { SB; asm volatile("s_waitcnt vmcnt(" #n ")\n\ts_barrier" ::: "memory"); SB; } while (0)
#define SYNCL(n) do { SB; asm volatile("s_waitcnt vmcnt(" #n ") lgkmcnt(0)\n\ts_barrier" ::: "memory"); SB; } while (0)
  u32 vW[3], vX[2][2];
#pragma unroll
  for (int i = 0; i < 3; ++i) { vW[i] = sm0 + lane * 16 + i * 65536; OPAQUE(vW[i]); }
#pragma unroll
  for (int t = 0; t < 2; ++t)
#pragma unroll
    for (int i = 0; i < 2; ++i) {
      const int lr = lane >> 2;
      vX[t][i] = sm0 + lr * 512 + (((2 * w + (t ^ rho)) ^ (lr & 7)) << 5) + (lane & 3) * 8 + i * 65536;
      OPAQUE(vX[t][i]);
    }
#define RDW(win, off) (*(LDS u32x4*)(ldsp(vW[win]) + (off)))
#define RDX(t, w0, o0, w1, o1) trfrag(ldsp(vX[t][w0]) + (o0), ldsp(vX[t][w1]) + (o1))
#define G2_BASES() do { _Pragma("unroll") for (int i_ = 0; i_ < 3; ++i_) { vW[i_] += kg * 1024; OPAQUE(vW[i_]); } } while (0)
#define MM(c, a, b) asm volatile("v_mfma_f32_16x16x32_bf16 %0, %1, %2, %0" : "+v"(c) : "v"(a), "v"(b))
#define MM0(c, a, b) asm volatile("v_mfma_f32_16x16x32_bf16 %0, %1, %2, 0" : "=&v"(c) : "v"(a), "v"(b))
#define SETTLE(a, b) asm volatile("s_nop 1" : "+v"(a), "+v"(b))
#define XPRE() asm volatile("s_nop 7\n\ts_nop 1" ::: "memory")
  float cs[3];
  const u32 sCS = WS_CS + (min(w, 5) * 96 + 3 * s) * 64;
#define CS_LD(j) do { u32 t_; asm volatile("s_add_u32 %1, %2, %3\n\tbuffer_load_dword %0, %4, %5, %1 offen sc1" \
    : "=v"(cs[j]), "=&s"(t_) : "s"(sCS), "i"((j) * 36864), "v"(fresh(vL16) >> 2), "s"(srd) : "memory", "scc"); } while (0)
#define CS_FIN(n) do { asm volatile("s_waitcnt vmcnt(" #n ")" : "+v"(cs[0]), "+v"(cs[1]), "+v"(cs[2]) :: "memory"); \
    *(LDS float*)ldsp((fresh(vL16) >> 2) + (sm0 + M_CS + w * 256)) = (cs[0] + cs[1]) + cs[2]; } while (0)
  u32 vCr;
#define CS_BASE() do { vCr = sm0 + M_CS + ((fresh(vL16) >> 4) & 48); OPAQUE(vCr); } while (0)
#define RDCS(off) (*(LDS f32x4*)(ldsp(vCr) + (off)))
  u32x4 ones = {0x3f803f80u, 0x3f803f80u, 0x3f803f80u, 0x3f803f80u};
  f32x4 gram;
  OPAQUE(ones);
  float nm[2];
#define MEAN_FIN() do { nm[0] = sm0_[0] * (-1.f / SD); nm[1] = sm1_[0] * (-1.f / SD); } while (0)
  u32 vRs;
#define RS_ADR() do { const u32 l_ = fresh(vL16) >> 4; vRs = sm0 + M_RS + ((l_ >> 4) == ((l_ >> 2) & 3) ? 16 * w + (l_ & 15) : 128 + l_) * 4; OPAQUE(vRs); } while (0)
#define RS_FIN() do { float q0_ = gram[0], q2_ = gram[2]; const float q1_ = gram[1], q3_ = gram[3]; \
    asm("v_cndmask_b32 %0, %0, %2, %4\n\tv_cndmask_b32 %1, %1, %3, %4\n\ts_nop 0\n\tv_cndmask_b32 %0, %0, %1, %5" \
        : "+v"(q0_), "+v"(q2_) : "v"(q1_), "v"(q3_), "s"(0xaaaaaaaaaaaaaaaaull), "s"(0xccccccccccccccccull)); \
    *(LDS float*)ldsp(vRs) = q0_ * (1.f / SD) - nm[0] * nm[0]; } while (0)
#define VR(r0, r3) "v[" #r0 ":" #r3 "]"
#define MMF(c, r0, r3, a, b) asm volatile("v_mfma_f32_16x16x32_bf16 %0, %1, %2, %0" : "+{" VR(r0, r3) "}"(c) : "v"(a), "v"(b))
#define MMF0(c, r0, r3, a, b) asm volatile("v_mfma_f32_16x16x32_bf16 %0, %1, %2, 0" : "=&{" VR(r0, r3) "}"(c) : "v"(a), "v"(b))
#define EL(c) "v"((float)c[0]), "v"((float)c[1]), "v"((float)c[2]), "v"((float)c[3])
#define CORR(a, r0, r1, r2, r3, c, t) asm volatile("v_fmac_f32 v" #r0 ", %1, %2\n\tv_fmac_f32 v" #r1 ", %1, %3\n\tv_fmac_f32 v" #r2 ", %1, %4\n\tv_fmac_f32 v" #r3 ", %1, %5" \
    : "+{" VR(r0, r3) "}"(a) : "v"(nm[t]), EL(c))
#define ADD4(a, r0, r1, r2, r3, c) asm volatile("v_add_f32 v" #r0 ", v" #r0 ", %1\n\tv_add_f32 v" #r1 ", v" #r1 ", %2\n\tv_add_f32 v" #r2 ", v" #r2 ", %3\n\tv_add_f32 v" #r3 ", v" #r3 ", %4" \
    : "+{" VR(r0, r3) "}"(a) : EL(c))
#define GATE(p, g, r0, r1, r2, r3, u) u32 p##a, p##b; asm volatile("v_mul_f32 v" #r0 ", v" #r0 ", %3\n\tv_mul_f32 v" #r1 ", v" #r1 ", %4\n\tv_mul_f32 v" #r2 ", v" #r2 ", %5\n\t" \
    "v_mul_f32 v" #r3 ", v" #r3 ", %6\n\tv_cvt_pk_bf16_f32 %0, v" #r0 ", v" #r1 "\n\tv_cvt_pk_bf16_f32 %1, v" #r2 ", v" #r3 \
    : "=v"(p##a), "=v"(p##b), "+{" VR(r0, r3) "}"(g) : EL(u)); const u32x2 p = {p##a, p##b}
#define PIN2(a, b) asm volatile("" : "+v"(a), "+v"(b))
#define PRIO(n) __builtin_amdgcn_s_setprio(n)
  u32 sH[2];
#pragma unroll
  for (int t = 0; t < 2; ++t) sH[t] = WS_HH + ((16 * pan + 2 * w + (t ^ rho)) * 144 + 9 * (s >> 1)) * 1024;
  const u32 sHf = (s & 1) * 4096, sHh = 8192 + (s & 1) * 512;
#define HST4(a, b, t, off) bst16_dev(u32x4{a[0], a[1], b[0], b[1]}, srd, vL16, sH[t] + sHf + (off))
#define HST2(a, t) __builtin_amdgcn_raw_buffer_store_b64(a, rsw, fresh(vL16) >> 1, sH[t] + sHh, 16)
  const u32 sFw = FH_OFF + (((pan * 4 + (w >> 1)) * 8 + (s >> 2)) * 2 + (w & 1)) * 64 + (s & 3) * 16;
  const u32 sFr = FH_OFF + ((pan * 4 + rg) * 8 + 2 * kap + kg) * 128;
#define FLAG(n, v, sF) do { asm volatile("s_waitcnt vmcnt(" #n ")" ::: "memory"); __builtin_amdgcn_raw_buffer_store_b32(T4 + (v), rsw, fresh(vL16) << 22, sF, 16); \
    asm volatile("" ::: "memory"); } while (0)
  u32 pl;
#define POLL(sF, vo, nl) asm volatile("s_bfm_b64 exec, " #nl ", 0\n\tbuffer_load_dword %0, %3, %1, %2 offen sc1\n\ts_mov_b64 exec, -1" \
    : "=v"(pl) : "s"(srd), "s"(sF), "v"(vo) : "memory")
  u64 nrdy;
#define CMP(n, v) asm volatile("s_waitcnt vmcnt(" #n ")\n\tv_subrev_u32 %0, %2, %0\n\tv_cmp_lt_u32 %1, %3, %0" : "+v"(pl), "=s"(nrdy) : "s"(T4 + (v)), "i"(3 - (v)) : "memory")
#define CHECK(v, sF, vo, nl) do { u32 c_; asm volatile("s_bfm_b64 vcc, " #nl ", 0\n\ts_and_b64 vcc, vcc, %7\n\ts_cbranch_scc1 .Lwait%=\n" \
    ".Lgo%=:\n\t.subsection 1\n" \
    ".Lwait%=:\n\ts_mov_b32 %1, 0\n\ts_bfm_b64 exec, " #nl ", 0\n" \
    ".Lmore%=:\n\ts_sleep 2\n\tbuffer_load_dword %0, %4, %2, %3 offen sc1\n\ts_waitcnt vmcnt(0)\n\tv_subrev_u32 %0, %5, %0\n\t" \
    "v_cmp_lt_u32 vcc, %6, %0\n\ts_add_u32 %1, %1, 1\n\ts_cmp_gt_u32 %1, 0x100000\n\ts_cbranch_scc1 .Lend%=\n\t" \
    "s_cmp_lg_u64 vcc, 0\n\ts_cbranch_scc1 .Lmore%=\n.Lend%=:\n\ts_mov_b64 exec, -1\n\ts_branch .Lgo%=\n\t.subsection 0" \
    : "=&v"(pl), "=&s"(c_) : "s"(srd), "s"(sF), "v"(vo), "s"(T4 + (v)), "i"(3 - (v)), "s"(nrdy) : "memory", "scc", "vcc"); } while (0)
  const u32 sTr = WS_FT + 144 * 64, vTr = vFt + (lane & 2) * (72 * 64);
#define POLLT() POLL(sTr, vTr, 4)
  const u32 sEr = bid == 0 ? FH_OFF + 65536 + w * 512 : FH_OFF + 65536 + kap * 1024, sEw = bid == 0 && w == 0 ? (u32)WS_EPOCH : 1u << 30;
#define EPOCH() do { __builtin_amdgcn_raw_buffer_store_b32(tag, rsw, fresh(vL16) << 22, sEw, 16); asm volatile("" ::: "memory"); } while (0)
  u32 sG[2];
#pragma unroll
  for (int t = 0; t < 2; ++t) sG[t] = WS_HH + ((16 * pan + 4 * rg + 2 * (t ^ kg) + rho) * 144 + 9 * (4 * kap + 2 * kg)) * 1024;
#define HLD4(h, t, off) do { u32 t_; asm volatile("s_add_u32 %1, %2, %3\n\tbuffer_load_dwordx4 %0, %4, %5, %1 offen sc1" \
    : "=v"(h), "=&s"(t_) : "s"(sG[t]), "i"(off), "v"(vL16), "s"(srd) : "memory", "scc"); } while (0)
#define HLD2(h, t, off) do { u32 t_; asm volatile("s_add_u32 %1, %2, %3\n\tbuffer_load_dwordx2 %0, %4, %5, %1 offen sc1" \
    : "=v"(h), "=&s"(t_) : "s"(sG[t]), "i"(off), "v"(fresh(vL16) >> 1), "s"(srd) : "memory", "scc"); } while (0)
#define HWAIT4(n, a, b) asm volatile("s_waitcnt vmcnt(" #n ")" : "+v"(a), "+v"(b) :: "memory")
#define HWAIT2(n, a, b, c, d) asm volatile("s_waitcnt vmcnt(" #n ")" : "+v"(a), "+v"(b), "+v"(c), "+v"(d) :: "memory")
#define CAT(a, b) (u32x4{a[0], a[1], b[0], b[1]})
  const int tok0 = 256 * pan + 64 * rg + 32 * kg + 16 * rho;
  const u32 sR = WS_HT + 32 * (4 * kap + cc) * HTS + tok0 * 2, sR8 = WS_HT + (512 + 16 * cc) * HTS + tok0 * 2;
  u32 vR;
#define RES_SRC() do { const u32 l_ = fresh(vL16); vR = (l_ >> 5) * HTS + (l_ & 16); OPAQUE(vR); } while (0)
#define RES_LD8(li) asm volatile("s_add_u32 m0, %0, %1\n\ts_bfm_b64 exec, 32, 0\n\tbuffer_load_dwordx4 %2, %3, %4 offen sc1 lds\n\ts_mov_b64 exec, -1" \
    :: "s"(sL), "i"(li), "v"(vR), "s"(srd), "s"(sR8) : "memory", "scc")
#define RES_RD(n, a0, a8) asm volatile("s_waitcnt vmcnt(" #n ")" ::: "memory"); \
    const u32 vRr = sL + (fresh(vL16) >> 1); \
    u32x4 res = trfrag(ldsp(vRr + (vRr & 384) + (a0)), ldsp(vRr + (vRr & 384) + (a0)) + 128); \
    u32x2 res8 = __builtin_bit_cast(u32x2, trread(ldsp(vRr + (a8))))
#define XW(v, win, off) (*(LDS f32x4*)(ldsp(sL + vL16 + (win) * 65536) + (off)) = v)
  const u32 box = ((pan * 2 + rho) * 4 + cc) * 8 + w;
  const u32 sIn = WS_PART + (box * 4 + kap) * PTS, sIw = FH_OFF + 32768 + box * 64 + kap * 16, sIr = FH_OFF + 32768 + box * 64;
  const u32 sIn8 = rg == kap ? sIn + 3072 : 1u << 30;
#define PREFILL(off) bst16_dev(u32x4{~0u, ~0u, ~0u, ~0u}, srd, vL16, (off) < 3072 ? sIn + (off) : sIn8 + ((off) - 3072))
#define PACK(a) (u32x2{min(pkbf(a[0], a[1]), 0xfffffffeu), min(pkbf(a[2], a[3]), 0xfffffffeu)})
#define SEND4(j, a, b) bst16_dev(CAT(a, b), srd, vL16, WS_PART + (box * 4 + ((kap + 1 + (j)) & 3)) * PTS + (2 - (j)) * 1024)
#define SEND2(a) __builtin_amdgcn_raw_buffer_store_b64(a, rsw, fresh(vL16) >> 1, \
    WS_PART + (box * 4 + rg) * PTS + 3072 + ((kap - rg - 1) & 3) * 512, 16); asm volatile("" ::: "memory")
  u32x4 q[3];
  u32x2 q8[3];
#define INBOX_LD() asm volatile("buffer_load_dwordx4 %0, %6, %8, %9 offen sc1\n\tbuffer_load_dwordx4 %1, %6, %8, %9 offen offset:1024 sc1\n\t" \
    "buffer_load_dwordx4 %2, %6, %8, %9 offen offset:2048 sc1\n\tbuffer_load_dwordx2 %3, %7, %8, %10 offen sc1\n\t" \
    "buffer_load_dwordx2 %4, %7, %8, %10 offen offset:512 sc1\n\tbuffer_load_dwordx2 %5, %7, %8, %10 offen offset:1024 sc1" \
    : "=&v"(q[0]), "=&v"(q[1]), "=&v"(q[2]), "=&v"(q8[0]), "=&v"(q8[1]), "=&v"(q8[2]) \
    : "v"(vL16), "v"(fresh(vL16) >> 1), "s"(srd), "s"(sIn), "s"(sIn8) : "memory")
#define XSYNC() asm volatile("s_waitcnt lgkmcnt(0)\n\ts_barrier" ::: "memory")
#define XR(win, off) (*(LDS f32x4*)(ldsp(sm0 + (w ^ 4) * 1024 + vL16 + (win) * 65536) + (off)))

  {
    asm volatile("" :: "s"(sWd), "s"(sWm), "s"(sL4), "s"(sWw), "s"(sWr), "s"(sCS), "s"(sH[0]), "s"(sH[1]), "s"(sHf), "s"(sHh), "s"(sFw), "s"(sFr),
                 "s"(sEr), "s"(sEw), "s"(sG[0]), "s"(sG[1]), "s"(sR), "s"(sR8), "s"(sIn), "s"(sIw), "s"(sIr), "s"(sIn8), "s"(box), "v"(vTr));
    u32 d_, c_;
    u64 ex_;
    asm volatile("s_barrier\n\ts_cmp_ge_u32 %6, 192\n\ts_cbranch_scc1 3f\n\ts_sleep 6\n3:\n\ts_mov_b32 %1, 0\n\ts_mov_b64 %2, exec\n\ts_mov_b64 exec, 0x3ffffff\n"
                 "1:\n\tbuffer_load_dword %0, %3, %4, 0 offen sc1\n\ts_waitcnt vmcnt(0)\n\tv_cmpx_ne_u32 vcc, %5, %0\n\ts_nop 1\n\t"
                 "s_cbranch_execz 2f\n\ts_add_u32 %1, %1, 1\n\ts_cmp_gt_u32 %1, 0x100000\n\ts_cbranch_scc1 2f\n\t"
                 "s_sleep 2\n\ts_branch 1b\n2:\n\ts_mov_b64 exec, %2\n\ts_waitcnt lgkmcnt(0)\n\ts_barrier"
                 : "=&v"(d_), "=&s"(c_), "=&s"(ex_)
                 : "v"(vP), "s"(srd), "s"(T4), "s"(bid) : "memory", "scc", "vcc");
  }
DMA(sW1, 0, vL16, 98304);
DMA(sX, 0 * HTS, vXs, 0);
DMA(sX, 4 * HTS, vXs, 8192);
DMA(sX, 32 * HTS, vXs, 16384);
DMA(sX, 36 * HTS, vXs, 24576);
SB;
DMA(sW1, 8192, vL16, 106496);
DMA(sX, 64 * HTS, vXs, 32768);
DMA(sX, 68 * HTS, vXs, 40960);

SYNC(5);
const u32x4 w0 = RDW(1, 32768);
const u32x4 w1 = RDW(1, 33792);
const u32x4 w2 = RDW(1, 34816);
const u32x4 w3 = RDW(1, 35840);
const u32x4 x0_0 = RDX(0, 0, 0, 0, 8192);
const u32x4 x0_1 = RDX(1, 0, 0, 0, 8192);
SB;

PRIO(3);
const u32x4 w4 = RDW(1, 36864);
f32x4 a0_0; MMF0(a0_0, 208, 211, w0, x0_0);
DMA(sW1, 16384, vL16, 114688);
f32x4 a0_1; MMF0(a0_1, 212, 215, w0, x0_1);
f32x4 sm0_, sm1_; MM0(sm0_, ones, x0_0); MM0(sm1_, ones, x0_1);
SB;
SYNC(3);
PRIO(3);
const u32x4 w5 = RDW(1, 37888);
const u32x4 x1_0 = RDX(0, 0, 16384, 0, 24576);
f32x4 a1_0; MMF0(a1_0, 216, 219, w1, x0_0);
DMA(sX, 96 * HTS, vXs, 49152);
f32x4 a1_1; MMF0(a1_1, 220, 223, w1, x0_1);
SB;
PRIO(2);
const u32x4 w6 = RDW(1, 38912);
f32x4 a2_0; MMF0(a2_0, 224, 227, w2, x0_0);
DMA(sX, 100 * HTS, vXs, 57344);
f32x4 a2_1; MMF0(a2_1, 228, 231, w2, x0_1);
SB;
const u32x4 w7 = RDW(1, 39936);
const u32x4 x1_1 = RDX(1, 0, 16384, 0, 24576);
f32x4 a3_0; MMF0(a3_0, 232, 235, w3, x0_0);
DMA(sX, 128 * HTS, vXs, 65536);
f32x4 a3_1; MMF0(a3_1, 236, 239, w3, x0_1);
SB;
PRIO(1);
const u32x4 w8 = RDW(1, 40960);
f32x4 a4_0; MMF0(a4_0, 240, 243, w4, x0_0);
f32x4 a4_1; MMF0(a4_1, 244, 247, w4, x0_1);
SB;
const u32x4 w9 = RDW(1, 41984);
f32x4 a5_0; MMF0(a5_0, 248, 251, w5, x0_0);
DMA(sX, 132 * HTS, vXs, 73728);
f32x4 a5_1; MMF0(a5_1, 252, 255, w5, x0_1);
SB;
PRIO(0);
const u32x4 w10 = RDW(1, 43008);
MMF(a0_0, 208, 211, w6, x1_0);
MMF(a0_1, 212, 215, w6, x1_1);
MM(sm0_, ones, x1_0); MM(sm1_, ones, x1_1);
SB;
SYNC(4);
PRIO(3);
const u32x4 w11 = RDW(1, 44032);
const u32x4 x2_0 = RDX(0, 0, 32768, 0, 40960);
MMF(a1_0, 216, 219, w7, x1_0);
DMA(sW1, 24576, vL16, 122880);
MMF(a1_1, 220, 223, w7, x1_1);
SB;
PRIO(2);
const u32x4 w12 = RDW(1, 45056);
MMF(a2_0, 224, 227, w8, x1_0);
MMF(a2_1, 228, 231, w8, x1_1);
SB;
const u32x4 w13 = RDW(1, 46080);
const u32x4 x2_1 = RDX(1, 0, 32768, 0, 40960);
MMF(a3_0, 232, 235, w9, x1_0);
DMA(sX, 160 * HTS, vXs, 81920);
MMF(a3_1, 236, 239, w9, x1_1);
SB;
PRIO(1);
const u32x4 w14 = RDW(1, 47104);
MMF(a4_0, 240, 243, w10, x1_0);
MMF(a4_1, 244, 247, w10, x1_1);
SB;
const u32x4 w15 = RDW(1, 48128);
MMF(a5_0, 248, 251, w11, x1_0);
DMA(sX, 164 * HTS, vXs, 90112);
MMF(a5_1, 252, 255, w11, x1_1);
SB;
PRIO(0);
const u32x4 w16 = RDW(1, 49152);
MMF(a0_0, 208, 211, w12, x2_0);
MMF(a0_1, 212, 215, w12, x2_1);
MM(sm0_, ones, x2_0); MM(sm1_, ones, x2_1);
SB;
SYNC(5);
PRIO(3);
const u32x4 w17 = RDW(1, 50176);
const u32x4 x3_0 = RDX(0, 0, 49152, 0, 57344);
MMF(a1_0, 216, 219, w13, x2_0);
DMA(sW1, 32768, vL16, 131072);
MMF(a1_1, 220, 223, w13, x2_1);
SB;
PRIO(2);
const u32x4 w18 = RDW(1, 51200);
MMF(a2_0, 224, 227, w14, x2_0);
MMF(a2_1, 228, 231, w14, x2_1);
SB;
const u32x4 w19 = RDW(1, 52224);
const u32x4 x3_1 = RDX(1, 0, 49152, 0, 57344);
MMF(a3_0, 232, 235, w15, x2_0);
DMA(sX, 192 * HTS, vXs, 0);
MMF(a3_1, 236, 239, w15, x2_1);
SB;
PRIO(1);
const u32x4 w20 = RDW(1, 53248);
MMF(a4_0, 240, 243, w16, x2_0);
MMF(a4_1, 244, 247, w16, x2_1);
SB;
const u32x4 w21 = RDW(1, 54272);
MMF(a5_0, 248, 251, w17, x2_0);
DMA(sX, 196 * HTS, vXs, 8192);
MMF(a5_1, 252, 255, w17, x2_1);
SB;
PRIO(0);
const u32x4 w22 = RDW(1, 55296);
MMF(a0_0, 208, 211, w18, x3_0);
MMF(a0_1, 212, 215, w18, x3_1);
MM(sm0_, ones, x3_0); MM(sm1_, ones, x3_1);
SB;
SYNC(5);
PRIO(3);
const u32x4 w23 = RDW(1, 56320);
const u32x4 x4_0 = RDX(0, 1, 0, 1, 8192);
MMF(a1_0, 216, 219, w19, x3_0);
DMA(sW1, 40960, vL16, 139264);
MMF(a1_1, 220, 223, w19, x3_1);
SB;
PRIO(2);
const u32x4 w24 = RDW(1, 57344);
MMF(a2_0, 224, 227, w20, x3_0);
MMF(a2_1, 228, 231, w20, x3_1);
SB;
const u32x4 w25 = RDW(1, 58368);
const u32x4 x4_1 = RDX(1, 1, 0, 1, 8192);
MMF(a3_0, 232, 235, w21, x3_0);
DMA(sX, 224 * HTS, vXs, 16384);
MMF(a3_1, 236, 239, w21, x3_1);
SB;
PRIO(1);
const u32x4 w26 = RDW(1, 59392);
MMF(a4_0, 240, 243, w22, x3_0);
MMF(a4_1, 244, 247, w22, x3_1);
SB;
const u32x4 w27 = RDW(1, 60416);
MMF(a5_0, 248, 251, w23, x3_0);
DMA(sX, 228 * HTS, vXs, 24576);
MMF(a5_1, 252, 255, w23, x3_1);
SB;
PRIO(0);
const u32x4 w28 = RDW(1, 61440);
MMF(a0_0, 208, 211, w24, x4_0);
MMF(a0_1, 212, 215, w24, x4_1);
MM(sm0_, ones, x4_0); MM(sm1_, ones, x4_1);
SB;
SYNC(5);
PRIO(3);
const u32x4 w29 = RDW(1, 62464);
const u32x4 x5_0 = RDX(0, 1, 16384, 1, 24576);
MMF(a1_0, 216, 219, w25, x4_0);
DMA(sX, 256 * HTS, vXs, 32768);
MMF(a1_1, 220, 223, w25, x4_1);
SB;
PRIO(2);
const u32x4 w30 = RDW(1, 63488);
MMF(a2_0, 224, 227, w26, x4_0);
MMF(a2_1, 228, 231, w26, x4_1);
SB;
const u32x4 w31 = RDW(1, 64512);
const u32x4 x5_1 = RDX(1, 1, 16384, 1, 24576);
MMF(a3_0, 232, 235, w27, x4_0);
DMA(sX, 260 * HTS, vXs, 40960);
MMF(a3_1, 236, 239, w27, x4_1);
SB;
PRIO(1);
const u32x4 w32 = RDW(2, 0);
MMF(a4_0, 240, 243, w28, x4_0);
MMF(a4_1, 244, 247, w28, x4_1);
SB;
const u32x4 w33 = RDW(2, 1024);
MMF(a5_0, 248, 251, w29, x4_0);
DMA(sW1, 49152, vL16, 147456);
MMF(a5_1, 252, 255, w29, x4_1);
SB;
PRIO(0);
const u32x4 w34 = RDW(2, 2048);
MMF(a0_0, 208, 211, w30, x5_0);
MMF(a0_1, 212, 215, w30, x5_1);
MM(sm0_, ones, x5_0); MM(sm1_, ones, x5_1);
SB;
SYNC(5);
PRIO(3);
const u32x4 w35 = RDW(2, 3072);
const u32x4 x6_0 = RDX(0, 0, 0, 0, 8192);
MMF(a1_0, 216, 219, w31, x5_0);
DMA(sX, 288 * HTS, vXs, 49152);
MMF(a1_1, 220, 223, w31, x5_1);
SB;
PRIO(2);
const u32x4 w36 = RDW(2, 4096);
MMF(a2_0, 224, 227, w32, x5_0);
MMF(a2_1, 228, 231, w32, x5_1);
SB;
const u32x4 w37 = RDW(2, 5120);
const u32x4 x6_1 = RDX(1, 0, 0, 0, 8192);
MMF(a3_0, 232, 235, w33, x5_0);
DMA(sX, 292 * HTS, vXs, 57344);
MMF(a3_1, 236, 239, w33, x5_1);
SB;
PRIO(1);
const u32x4 w38 = RDW(2, 6144);
MMF(a4_0, 240, 243, w34, x5_0);
MMF(a4_1, 244, 247, w34, x5_1);
SB;
const u32x4 w39 = RDW(2, 7168);
MMF(a5_0, 248, 251, w35, x5_0);
DMA(sW1, 57344, vL16, 98304);
MMF(a5_1, 252, 255, w35, x5_1);
SB;
PRIO(0);
const u32x4 w40 = RDW(2, 8192);
MMF(a0_0, 208, 211, w36, x6_0);
MMF(a0_1, 212, 215, w36, x6_1);
MM(sm0_, ones, x6_0); MM(sm1_, ones, x6_1);
SB;
SYNC(6);
PRIO(3);
const u32x4 w41 = RDW(2, 9216);
const u32x4 x7_0 = RDX(0, 0, 16384, 0, 24576);
MMF(a1_0, 216, 219, w37, x6_0);
DMA(sX, 320 * HTS, vXs, 65536);
MMF(a1_1, 220, 223, w37, x6_1);
SB;
PRIO(2);
const u32x4 w42 = RDW(2, 10240);
MMF(a2_0, 224, 227, w38, x6_0);
POLLT();
MMF(a2_1, 228, 231, w38, x6_1);
SB;
const u32x4 w43 = RDW(2, 11264);
const u32x4 x7_1 = RDX(1, 0, 16384, 0, 24576);
MMF(a3_0, 232, 235, w39, x6_0);
DMA(sX, 324 * HTS, vXs, 73728);
MMF(a3_1, 236, 239, w39, x6_1);
SB;
PRIO(1);
const u32x4 w44 = RDW(2, 12288);
MMF(a4_0, 240, 243, w40, x6_0);
MMF(a4_1, 244, 247, w40, x6_1);
SB;
const u32x4 w45 = RDW(2, 13312);
MMF(a5_0, 248, 251, w41, x6_0);
DMA(sW1, 65536, vL16, 106496);
MMF(a5_1, 252, 255, w41, x6_1);
SB;
PRIO(0);
const u32x4 w46 = RDW(2, 14336);
MMF(a0_0, 208, 211, w42, x7_0);
MMF(a0_1, 212, 215, w42, x7_1);
MM(sm0_, ones, x7_0); MM(sm1_, ones, x7_1);
SB;
SYNC(7);
PRIO(3);
const u32x4 w47 = RDW(2, 15360);
const u32x4 x8_0 = RDX(0, 0, 32768, 0, 40960);
MMF(a1_0, 216, 219, w43, x7_0);
DMA(sX, 352 * HTS, vXs, 81920);
MMF(a1_1, 220, 223, w43, x7_1);
SB;
PRIO(2);
const u32x4 w48 = RDW(2, 16384);
MMF(a2_0, 224, 227, w44, x7_0);
MMF(a2_1, 228, 231, w44, x7_1);
SB;
const u32x4 w49 = RDW(2, 17408);
const u32x4 x8_1 = RDX(1, 0, 32768, 0, 40960);
MMF(a3_0, 232, 235, w45, x7_0);
DMA(sX, 356 * HTS, vXs, 90112);
MMF(a3_1, 236, 239, w45, x7_1);
SB;
PRIO(1);
const u32x4 w50 = RDW(2, 18432);
MMF(a4_0, 240, 243, w46, x7_0);
MMF(a4_1, 244, 247, w46, x7_1);
SB;
const u32x4 w51 = RDW(2, 19456);
MMF(a5_0, 248, 251, w47, x7_0);
DMA(sX, 384 * HTS, vXs, 0);
MMF(a5_1, 252, 255, w47, x7_1);
SB;
PRIO(0);
const u32x4 w52 = RDW(2, 20480);
MMF(a0_0, 208, 211, w48, x8_0);
MMF(a0_1, 212, 215, w48, x8_1);
MM(sm0_, ones, x8_0); MM(sm1_, ones, x8_1);
SB;
SYNC(7);
PRIO(3);
const u32x4 w53 = RDW(2, 21504);
const u32x4 x9_0 = RDX(0, 0, 49152, 0, 57344);
MMF(a1_0, 216, 219, w49, x8_0);
DMA(sX, 388 * HTS, vXs, 8192);
MMF(a1_1, 220, 223, w49, x8_1);
SB;
PRIO(2);
const u32x4 w54 = RDW(2, 22528);
MMF(a2_0, 224, 227, w50, x8_0);
MMF(a2_1, 228, 231, w50, x8_1);
SB;
const u32x4 w55 = RDW(2, 23552);
const u32x4 x9_1 = RDX(1, 0, 49152, 0, 57344);
MMF(a3_0, 232, 235, w51, x8_0);
CMP(6, 0);
DMA(sW1, 73728, vL16, 114688);
MMF(a3_1, 236, 239, w51, x8_1);
SB;
PRIO(1);
const u32x4 w56 = RDW(1, 32768);
MMF(a4_0, 240, 243, w52, x8_0);
MMF(a4_1, 244, 247, w52, x8_1);
SB;
const u32x4 w57 = RDW(1, 33792);
MMF(a5_0, 248, 251, w53, x8_0);
CHECK(0, sTr, vTr, 4);
DMA(sX, 416 * HTS, vXs, 16384);
MMF(a5_1, 252, 255, w53, x8_1);
SB;
PRIO(0);
const u32x4 w58 = RDW(1, 34816);
MMF(a0_0, 208, 211, w54, x9_0);
MMF(a0_1, 212, 215, w54, x9_1);
MM(sm0_, ones, x9_0); MM(sm1_, ones, x9_1);
SB;
SYNC(6);
PRIO(3);
const u32x4 w59 = RDW(1, 35840);
const u32x4 x10_0 = RDX(0, 1, 0, 1, 8192);
MMF(a1_0, 216, 219, w55, x9_0);
CS_LD(0);
DMA(sX, 420 * HTS, vXs, 24576);
MMF(a1_1, 220, 223, w55, x9_1);
SB;
PRIO(2);
const u32x4 w60 = RDW(1, 36864);
MMF(a2_0, 224, 227, w56, x9_0);
MMF(a2_1, 228, 231, w56, x9_1);
SB;
const u32x4 w61 = RDW(1, 37888);
const u32x4 x10_1 = RDX(1, 1, 0, 1, 8192);
MMF(a3_0, 232, 235, w57, x9_0);
CS_LD(1);
DMA(sW1, 81920, vL16, 122880);
MMF(a3_1, 236, 239, w57, x9_1);
SB;
PRIO(1);
const u32x4 w62 = RDW(1, 38912);
MMF(a4_0, 240, 243, w58, x9_0);
MMF(a4_1, 244, 247, w58, x9_1);
SB;
const u32x4 w63 = RDW(1, 39936);
MMF(a5_0, 248, 251, w59, x9_0);
CS_LD(2);
DMA(sX, 448 * HTS, vXs, 32768);
MMF(a5_1, 252, 255, w59, x9_1);
SB;
PRIO(0);
const u32x4 w64 = RDW(1, 40960);
MMF(a0_0, 208, 211, w60, x10_0);
MMF(a0_1, 212, 215, w60, x10_1);
MM(sm0_, ones, x10_0); MM(sm1_, ones, x10_1);
SB;
SYNC(10);
PRIO(3);
const u32x4 w65 = RDW(1, 41984);
const u32x4 x11_0 = RDX(0, 1, 16384, 1, 24576);
MMF(a1_0, 216, 219, w61, x10_0);
DMA(sX, 452 * HTS, vXs, 40960);
MMF(a1_1, 220, 223, w61, x10_1);
SB;
PRIO(2);
const u32x4 w66 = RDW(1, 43008);
MMF(a2_0, 224, 227, w62, x10_0);
MMF(a2_1, 228, 231, w62, x10_1);
SB;
const u32x4 w67 = RDW(1, 44032);
const u32x4 x11_1 = RDX(1, 1, 16384, 1, 24576);
MMF(a3_0, 232, 235, w63, x10_0);
DMA(sW1, 90112, vL16, 131072);
MMF(a3_1, 236, 239, w63, x10_1);
SB;
PRIO(1);
const u32x4 w68 = RDW(1, 45056);
MMF(a4_0, 240, 243, w64, x10_0);
MMF(a4_1, 244, 247, w64, x10_1);
SB;
const u32x4 w69 = RDW(1, 46080);
MMF(a5_0, 248, 251, w65, x10_0);
DMA(sX, 480 * HTS, vXs, 49152);
MMF(a5_1, 252, 255, w65, x10_1);
SB;
PRIO(0);
const u32x4 w70 = RDW(1, 47104);
MMF(a0_0, 208, 211, w66, x11_0);
MMF(a0_1, 212, 215, w66, x11_1);
MM(sm0_, ones, x11_0); MM(sm1_, ones, x11_1);
SB;
SYNC(10);
PRIO(3);
const u32x4 w71 = RDW(1, 48128);
const u32x4 x12_0 = RDX(0, 0, 0, 0, 8192);
MMF(a1_0, 216, 219, w67, x11_0);
DMA(sX, 484 * HTS, vXs, 57344);
MMF(a1_1, 220, 223, w67, x11_1);
SB;
PRIO(2);
const u32x4 w72 = RDW(1, 49152);
MMF(a2_0, 224, 227, w68, x11_0);
MMF(a2_1, 228, 231, w68, x11_1);
SB;
const u32x4 w73 = RDW(1, 50176);
const u32x4 x12_1 = RDX(1, 0, 0, 0, 8192);
MMF(a3_0, 232, 235, w69, x11_0);
DMA(sX, 512 * HTS, vXs, 65536);
MMF(a3_1, 236, 239, w69, x11_1);
SB;
PRIO(1);
const u32x4 w74 = RDW(1, 51200);
MMF(a4_0, 240, 243, w70, x11_0);
MMF(a4_1, 244, 247, w70, x11_1);
SB;
const u32x4 w75 = RDW(1, 52224);
MMF(a5_0, 248, 251, w71, x11_0);
DMA(sX, 516 * HTS, vXs, 73728);
MMF(a5_1, 252, 255, w71, x11_1);
SB;
PRIO(0);
const u32x4 w76 = RDW(1, 53248);
MMF(a0_0, 208, 211, w72, x12_0);
MMF(a0_1, 212, 215, w72, x12_1);
MM(sm0_, ones, x12_0); MM(sm1_, ones, x12_1);
SB;
SYNC(8);
PRIO(3);
const u32x4 w77 = RDW(1, 54272);
const u32x4 x13_0 = RDX(0, 0, 16384, 0, 24576);
MMF(a1_0, 216, 219, w73, x12_0);
DMA(sW1, 98304, vL16, 139264);
MMF(a1_1, 220, 223, w73, x12_1);
SB;
PRIO(2);
const u32x4 w78 = RDW(1, 55296);
MMF(a2_0, 224, 227, w74, x12_0);
MMF(a2_1, 228, 231, w74, x12_1);
SB;
const u32x4 w79 = RDW(1, 56320);
const u32x4 x13_1 = RDX(1, 0, 16384, 0, 24576);
MMF(a3_0, 232, 235, w75, x12_0);
DMA(sX, 544 * HTS, vXs, 81920);
MMF(a3_1, 236, 239, w75, x12_1);
SB;
PRIO(1);
const u32x4 w80 = RDW(1, 57344);
MMF(a4_0, 240, 243, w76, x12_0);
MMF(a4_1, 244, 247, w76, x12_1);
SB;
const u32x4 w81 = RDW(1, 58368);
MMF(a5_0, 248, 251, w77, x12_0);
DMA(sX, 548 * HTS, vXs, 90112);
MMF(a5_1, 252, 255, w77, x12_1);
SB;
PRIO(0);
const u32x4 w82 = RDW(1, 59392);
MMF(a0_0, 208, 211, w78, x13_0);
MMF(a0_1, 212, 215, w78, x13_1);
MM(sm0_, ones, x13_0); MM(sm1_, ones, x13_1);
SB;
SYNC(7);
PRIO(3);
const u32x4 w83 = RDW(1, 60416);
const u32x4 x14_0 = RDX(0, 0, 32768, 0, 40960);
MMF(a1_0, 216, 219, w79, x13_0);
DMA(sW1, 106496, vL16, 147456);
MMF(a1_1, 220, 223, w79, x13_1);
SB;
PRIO(2);
const u32x4 w84 = RDW(1, 61440);
MMF(a2_0, 224, 227, w80, x13_0);
CS_FIN(11);
MMF(a2_1, 228, 231, w80, x13_1);
SB;
const u32x4 w85 = RDW(1, 62464);
const u32x4 x14_1 = RDX(1, 0, 32768, 0, 40960);
MMF(a3_0, 232, 235, w81, x13_0);
DMA(sW1, 114688, vL16, 98304);
MMF(a3_1, 236, 239, w81, x13_1);
SB;
PRIO(1);
const u32x4 w86 = RDW(1, 63488);
MMF(a4_0, 240, 243, w82, x13_0);
MMF(a4_1, 244, 247, w82, x13_1);
SB;
const u32x4 w87 = RDW(1, 64512);
MMF(a5_0, 248, 251, w83, x13_0);
DMA(sW1, 122880, vL16, 106496);
MMF(a5_1, 252, 255, w83, x13_1);
SB;
PRIO(0);
const u32x4 w88 = RDW(2, 0);
MMF(a0_0, 208, 211, w84, x14_0);
MMF(a0_1, 212, 215, w84, x14_1);
MM(sm0_, ones, x14_0); MM(sm1_, ones, x14_1);
SB;
SYNC(8);
PRIO(3);
const u32x4 w89 = RDW(2, 1024);
const u32x4 x15_0 = RDX(0, 0, 49152, 0, 57344);
MMF(a1_0, 216, 219, w85, x14_0);
DMA(sW1, 131072, vL16, 114688);
MMF(a1_1, 220, 223, w85, x14_1);
SB;
PRIO(2);
const u32x4 w90 = RDW(2, 2048);
MMF(a2_0, 224, 227, w86, x14_0);
MMF(a2_1, 228, 231, w86, x14_1);
SB;
const u32x4 w91 = RDW(2, 3072);
const u32x4 x15_1 = RDX(1, 0, 49152, 0, 57344);
MMF(a3_0, 232, 235, w87, x14_0);
DMA(sW1, 139264, vL16, 32768);
MMF(a3_1, 236, 239, w87, x14_1);
SB;
PRIO(1);
const u32x4 w92 = RDW(2, 4096);
MMF(a4_0, 240, 243, w88, x14_0);
MMF(a4_1, 244, 247, w88, x14_1);
SB;
const u32x4 w93 = RDW(2, 5120);
MMF(a5_0, 248, 251, w89, x14_0);
DMA(sW1, 147456, vL16, 40960);
MMF(a5_1, 252, 255, w89, x14_1);
SB;
PRIO(0);
const u32x4 w94 = RDW(2, 6144);
MMF(a0_0, 208, 211, w90, x15_0);
MMF(a0_1, 212, 215, w90, x15_1);
MM(sm0_, ones, x15_0); MM(sm1_, ones, x15_1);
SB;
SYNC(8);
PRIO(3);
const u32x4 w95 = RDW(2, 7168);
const u32x4 x16_0 = RDX(0, 1, 0, 1, 8192);
MMF(a1_0, 216, 219, w91, x15_0);
DMA(sW1, 155648, vL16, 122880);
MMF(a1_1, 220, 223, w91, x15_1);
SB;
PRIO(2);
const u32x4 w96 = RDW(2, 8192);
MMF(a2_0, 224, 227, w92, x15_0);
MMF(a2_1, 228, 231, w92, x15_1);
SB;
const u32x4 w97 = RDW(2, 9216);
const u32x4 x16_1 = RDX(1, 1, 0, 1, 8192);
MMF(a3_0, 232, 235, w93, x15_0);
DMA(sW1, 163840, vL16, 49152);
MMF(a3_1, 236, 239, w93, x15_1);
SB;
PRIO(1);
const u32x4 w98 = RDW(2, 10240);
MMF(a4_0, 240, 243, w94, x15_0);
MMF(a4_1, 244, 247, w94, x15_1);
SB;
const u32x4 w99 = RDW(2, 11264);
MMF(a5_0, 248, 251, w95, x15_0);
DMA(sW1, 172032, vL16, 57344);
MMF(a5_1, 252, 255, w95, x15_1);
SB;
PRIO(0);
const u32x4 w100 = RDW(2, 12288);
MMF(a0_0, 208, 211, w96, x16_0);
MMF(a0_1, 212, 215, w96, x16_1);
MM(sm0_, ones, x16_0); MM(sm1_, ones, x16_1);
SB;
SYNC(6);
PRIO(3);
const u32x4 w101 = RDW(2, 13312);
const u32x4 x17_0 = RDX(0, 1, 16384, 1, 24576);
MMF(a1_0, 216, 219, w97, x16_0);
WT_SRC();
MMF(a1_1, 220, 223, w97, x16_1);
SB;
const u32x4 w102 = RDW(2, 14336);
MMF(a2_0, 224, 227, w98, x16_0);
WT_LD(0);
MMF(a2_1, 228, 231, w98, x16_1);
SB;
const u32x4 w103 = RDW(2, 15360);
const u32x4 x17_1 = RDX(1, 1, 16384, 1, 24576);
MMF(a3_0, 232, 235, w99, x16_0);
MMF(a3_1, 236, 239, w99, x16_1);
SB;
const u32x4 w104 = RDW(2, 16384);
MMF(a4_0, 240, 243, w100, x16_0);
WT_LD(1);
MMF(a4_1, 244, 247, w100, x16_1);
SB;
const u32x4 w105 = RDW(2, 17408);
MMF(a5_0, 248, 251, w101, x16_0);
MMF(a5_1, 252, 255, w101, x16_1);
SB;
const u32x4 w106 = RDW(2, 18432);
MMF(a0_0, 208, 211, w102, x17_0);
CS_BASE();
WT_LD(2);
MMF(a0_1, 212, 215, w102, x17_1);
MM(sm0_, ones, x17_0); MM(sm1_, ones, x17_1);
SB;
const u32x4 w107 = RDW(2, 19456);
MMF(a1_0, 216, 219, w103, x17_0);
MMF(a1_1, 220, 223, w103, x17_1);
SB;
const u32x4 w108 = RDW(2, 20480);
MMF(a2_0, 224, 227, w104, x17_0);
MEAN_FIN();
WT_LD(3);
MMF(a2_1, 228, 231, w104, x17_1);
SB;
const u32x4 w109 = RDW(2, 21504);
MMF(a3_0, 232, 235, w105, x17_0);
const f32x4 cg0 = RDCS(0), cu0 = RDCS(256);
MMF(a3_1, 236, 239, w105, x17_1);
SB;
const u32x4 w110 = RDW(2, 22528);
MMF(a4_0, 240, 243, w106, x17_0);
DMA(sW1, 180224, vL16, 131072);
MMF(a4_1, 244, 247, w106, x17_1);
SB;
const u32x4 w111 = RDW(2, 23552);
MMF(a5_0, 248, 251, w107, x17_0);
const f32x4 cg1 = RDCS(64), cu1 = RDCS(320);
MMF(a5_1, 252, 255, w107, x17_1);
SB; PRIO(0);
CORR(a0_0, 208, 209, 210, 211, cg0, 0); CORR(a1_0, 216, 217, 218, 219, cu0, 0); GATE(p0_0, a0_0, 208, 209, 210, 211, a1_0);
CORR(a0_1, 212, 213, 214, 215, cg0, 1); CORR(a1_1, 220, 221, 222, 223, cu0, 1); GATE(p0_1, a0_1, 212, 213, 214, 215, a1_1); SB;
const f32x4 cg2 = RDCS(128), cu2 = RDCS(384);
CORR(a2_0, 224, 225, 226, 227, cg1, 0); CORR(a3_0, 232, 233, 234, 235, cu1, 0); GATE(p1_0, a2_0, 224, 225, 226, 227, a3_0);
CORR(a2_1, 228, 229, 230, 231, cg1, 1); CORR(a3_1, 236, 237, 238, 239, cu1, 1); GATE(p1_1, a2_1, 228, 229, 230, 231, a3_1); SB;
CORR(a4_0, 240, 241, 242, 243, cg2, 0); CORR(a5_0, 248, 249, 250, 251, cu2, 0); GATE(p2_0, a4_0, 240, 241, 242, 243, a5_0);
CORR(a4_1, 244, 245, 246, 247, cg2, 1); CORR(a5_1, 252, 253, 254, 255, cu2, 1); GATE(p2_1, a4_1, 244, 245, 246, 247, a5_1); SB;
SB;

PRIO(2);
const u32x4 w112 = RDW(1, 32768);
MMF0(a0_0, 208, 211, w108, x0_0);
HST4(p0_0, p1_0, 0, 0);
MMF0(a0_1, 212, 215, w108, x0_1);
SB;
const u32x4 w113 = RDW(1, 33792);
MMF0(a1_0, 216, 219, w109, x0_0);
MMF0(a1_1, 220, 223, w109, x0_1);
SB;
const u32x4 w114 = RDW(1, 34816);
MMF0(a2_0, 224, 227, w110, x0_0);
MMF0(a2_1, 228, 231, w110, x0_1);
SB;
const u32x4 w115 = RDW(1, 35840);
MMF0(a3_0, 232, 235, w111, x0_0);
MMF0(a3_1, 236, 239, w111, x0_1);
SB;
const u32x4 w116 = RDW(1, 36864);
MMF0(a4_0, 240, 243, w112, x0_0);
DMA(sW1, 188416, vL16, 65536);
MMF0(a4_1, 244, 247, w112, x0_1);
SB;
const u32x4 w117 = RDW(1, 37888);
MMF0(a5_0, 248, 251, w113, x0_0);
MMF0(a5_1, 252, 255, w113, x0_1);
SB;
PRIO(1);
const u32x4 w118 = RDW(1, 38912);
MMF(a0_0, 208, 211, w114, x1_0);
MMF(a0_1, 212, 215, w114, x1_1);
SB;
const u32x4 w119 = RDW(1, 39936);
MMF(a1_0, 216, 219, w115, x1_0);
MMF(a1_1, 220, 223, w115, x1_1);
SB;
const u32x4 w120 = RDW(1, 40960);
MMF(a2_0, 224, 227, w116, x1_0);
HST4(p0_1, p1_1, 1, 0);
MMF(a2_1, 228, 231, w116, x1_1);
SB;
const u32x4 w121 = RDW(1, 41984);
MMF(a3_0, 232, 235, w117, x1_0);
MMF(a3_1, 236, 239, w117, x1_1);
MM0(gram, x0_0, x0_0);
SB;
const u32x4 w122 = RDW(1, 43008);
MMF(a4_0, 240, 243, w118, x1_0);
MMF(a4_1, 244, 247, w118, x1_1);
SB;
const u32x4 w123 = RDW(1, 44032);
MMF(a5_0, 248, 251, w119, x1_0);
MMF(a5_1, 252, 255, w119, x1_1);
SB;
PRIO(0);
const u32x4 w124 = RDW(1, 45056);
MMF(a0_0, 208, 211, w120, x2_0);
WT_ADR(4);
DMA(sW1, 196608, vL16, 73728);
MMF(a0_1, 212, 215, w120, x2_1);
SB;
const u32x4 w125 = RDW(1, 46080);
MMF(a1_0, 216, 219, w121, x2_0);
const u32x4 wt0 = WT_RD(0);
MMF(a1_1, 220, 223, w121, x2_1);
SB;
SYNC(12);
PRIO(3);
const u32x4 w126 = RDW(1, 47104);
MMF(a2_0, 224, 227, w122, x2_0);
MMF(a2_1, 228, 231, w122, x2_1);
MM(gram, x1_0, x1_0);
SB;
const u32x4 w127 = RDW(1, 48128);
MMF(a3_0, 232, 235, w123, x2_0);
MMF(a3_1, 236, 239, w123, x2_1);
SB;
const u32x4 w128 = RDW(1, 49152);
MMF(a4_0, 240, 243, w124, x2_0);
WT_ST(wt0, 0);
MMF(a4_1, 244, 247, w124, x2_1);
SB;
const u32x4 w129 = RDW(1, 50176);
MMF(a5_0, 248, 251, w125, x2_0);
MMF(a5_1, 252, 255, w125, x2_1);
SB;
const u32x4 w130 = RDW(1, 51200);
MMF(a0_0, 208, 211, w126, x3_0);
MMF(a0_1, 212, 215, w126, x3_1);
SB;
const u32x4 w131 = RDW(1, 52224);
MMF(a1_0, 216, 219, w127, x3_0);
MMF(a1_1, 220, 223, w127, x3_1);
MM(gram, x2_0, x2_0);
SB;
PRIO(2);
const u32x4 w132 = RDW(1, 53248);
MMF(a2_0, 224, 227, w128, x3_0);
const u32x4 wt1 = WT_RD(1);
DMA(sW1, 204800, vL16, 139264);
MMF(a2_1, 228, 231, w128, x3_1);
SB;
const u32x4 w133 = RDW(1, 54272);
MMF(a3_0, 232, 235, w129, x3_0);
MMF(a3_1, 236, 239, w129, x3_1);
SB;
const u32x4 w134 = RDW(1, 55296);
MMF(a4_0, 240, 243, w130, x3_0);
MMF(a4_1, 244, 247, w130, x3_1);
SB;
const u32x4 w135 = RDW(1, 56320);
MMF(a5_0, 248, 251, w131, x3_0);
WT_ST(wt1, 1);
MMF(a5_1, 252, 255, w131, x3_1);
SB;
const u32x4 w136 = RDW(0, 32768);
MMF(a0_0, 208, 211, w132, x4_0);
MMF(a0_1, 212, 215, w132, x4_1);
MM(gram, x3_0, x3_0);
SB;
const u32x4 w137 = RDW(0, 33792);
MMF(a1_0, 216, 219, w133, x4_0);
MMF(a1_1, 220, 223, w133, x4_1);
SB;
const u32x4 w138 = RDW(0, 34816);
MMF(a2_0, 224, 227, w134, x4_0);
MMF(a2_1, 228, 231, w134, x4_1);
SB;
PRIO(1);
const u32x4 w139 = RDW(0, 35840);
MMF(a3_0, 232, 235, w135, x4_0);
const u32x4 wt2 = WT_RD(2);
DMA(sW1, 212992, vL16, 81920);
MMF(a3_1, 236, 239, w135, x4_1);
SB;
const u32x4 w140 = RDW(0, 36864);
MMF(a4_0, 240, 243, w136, x4_0);
MMF(a4_1, 244, 247, w136, x4_1);
SB;
const u32x4 w141 = RDW(0, 37888);
MMF(a5_0, 248, 251, w137, x4_0);
CS_BASE();
MMF(a5_1, 252, 255, w137, x4_1);
MM(gram, x4_0, x4_0);
SB;
const u32x4 w142 = RDW(0, 38912);
MMF(a0_0, 208, 211, w138, x5_0);
const f32x4 cc1_0 = RDCS(512);
WT_ST(wt2, 2);
MMF(a0_1, 212, 215, w138, x5_1);
SB;
const u32x4 w143 = RDW(0, 39936);
MMF(a1_0, 216, 219, w139, x5_0);
MMF(a1_1, 220, 223, w139, x5_1);
SB;
const u32x4 w144 = RDW(0, 40960);
MMF(a2_0, 224, 227, w140, x5_0);
MMF(a2_1, 228, 231, w140, x5_1);
SB;
const u32x4 w145 = RDW(0, 41984);
MMF(a3_0, 232, 235, w141, x5_0);
CORR(a0_0, 208, 209, 210, 211, cc1_0, 0);
MMF(a3_1, 236, 239, w141, x5_1);
SB;
PRIO(0);
const u32x4 w146 = RDW(0, 43008);
MMF(a4_0, 240, 243, w142, x5_0);
CORR(a0_1, 212, 213, 214, 215, cc1_0, 1);
const u32x4 wt3 = WT_RD(3);
DMA(sW1, 221184, vL16, 90112);
MMF(a4_1, 244, 247, w142, x5_1);
MM(gram, x5_0, x5_0);
SB;
const u32x4 w147 = RDW(0, 44032);
MMF(a5_0, 248, 251, w143, x5_0);
MMF(a5_1, 252, 255, w143, x5_1);
SB;
SYNC(15);
PRIO(3);
const u32x4 w148 = RDW(0, 45056);
MMF(a0_0, 208, 211, w144, x6_0);
MMF(a0_1, 212, 215, w144, x6_1);
SB;
const u32x4 w149 = RDW(0, 46080);
MMF(a1_0, 216, 219, w145, x6_0);
const f32x4 cc1_1 = RDCS(768);
FLAG(7, 1, sFw);
WT_ST(wt3, 3);
MMF(a1_1, 220, 223, w145, x6_1);
SB;
const u32x4 w150 = RDW(0, 47104);
MMF(a2_0, 224, 227, w146, x6_0);
MMF(a2_1, 228, 231, w146, x6_1);
SB;
const u32x4 w151 = RDW(0, 48128);
MMF(a3_0, 232, 235, w147, x6_0);
MMF(a3_1, 236, 239, w147, x6_1);
MM(gram, x6_0, x6_0);
SB;
const u32x4 w152 = RDW(1, 57344);
MMF(a4_0, 240, 243, w148, x6_0);
CORR(a1_0, 216, 217, 218, 219, cc1_1, 0);
MMF(a4_1, 244, 247, w148, x6_1);
SB;
const u32x4 w153 = RDW(1, 58368);
MMF(a5_0, 248, 251, w149, x6_0);
CORR(a1_1, 220, 221, 222, 223, cc1_1, 1);
DMA(sW1, 229376, vL16, 147456);
MMF(a5_1, 252, 255, w149, x6_1);
SB;
PRIO(2);
const u32x4 w154 = RDW(1, 59392);
MMF(a0_0, 208, 211, w150, x7_0);
MMF(a0_1, 212, 215, w150, x7_1);
SB;
const u32x4 w155 = RDW(1, 60416);
MMF(a1_0, 216, 219, w151, x7_0);
MMF(a1_1, 220, 223, w151, x7_1);
SB;
const u32x4 w156 = RDW(1, 61440);
MMF(a2_0, 224, 227, w152, x7_0);
const f32x4 cc1_2 = RDCS(576);
MMF(a2_1, 228, 231, w152, x7_1);
MM(gram, x7_0, x7_0);
SB;
const u32x4 w157 = RDW(1, 62464);
MMF(a3_0, 232, 235, w153, x7_0);
DMA(sW1, 237568, vL16, 98304);
MMF(a3_1, 236, 239, w153, x7_1);
SB;
const u32x4 w158 = RDW(1, 63488);
MMF(a4_0, 240, 243, w154, x7_0);
MMF(a4_1, 244, 247, w154, x7_1);
SB;
const u32x4 w159 = RDW(1, 64512);
MMF(a5_0, 248, 251, w155, x7_0);
CORR(a2_0, 224, 225, 226, 227, cc1_2, 0);
MMF(a5_1, 252, 255, w155, x7_1);
SB;
const u32x4 w160 = RDW(0, 49152);
MMF(a0_0, 208, 211, w156, x8_0);
CORR(a2_1, 228, 229, 230, 231, cc1_2, 1);
PREFILL(0);
MMF(a0_1, 212, 215, w156, x8_1);
SB;
PRIO(1);
const u32x4 w161 = RDW(0, 50176);
MMF(a1_0, 216, 219, w157, x8_0);
MMF(a1_1, 220, 223, w157, x8_1);
MM(gram, x8_0, x8_0);
SB;
const u32x4 w162 = RDW(0, 51200);
MMF(a2_0, 224, 227, w158, x8_0);
MMF(a2_1, 228, 231, w158, x8_1);
SB;
const u32x4 w163 = RDW(0, 52224);
MMF(a3_0, 232, 235, w159, x8_0);
const f32x4 cc1_3 = RDCS(832);
MMF(a3_1, 236, 239, w159, x8_1);
SB;
const u32x4 w164 = RDW(0, 53248);
MMF(a4_0, 240, 243, w160, x8_0);
DMA(sW1, 245760, vL16, 106496);
MMF(a4_1, 244, 247, w160, x8_1);
SB;
const u32x4 w165 = RDW(0, 54272);
MMF(a5_0, 248, 251, w161, x8_0);
MMF(a5_1, 252, 255, w161, x8_1);
SB;
const u32x4 w166 = RDW(0, 55296);
MMF(a0_0, 208, 211, w162, x9_0);
CORR(a3_0, 232, 233, 234, 235, cc1_3, 0);
MMF(a0_1, 212, 215, w162, x9_1);
MM(gram, x9_0, x9_0);
SB;
const u32x4 w167 = RDW(0, 56320);
MMF(a1_0, 216, 219, w163, x9_0);
CORR(a3_1, 236, 237, 238, 239, cc1_3, 1);
MMF(a1_1, 220, 223, w163, x9_1);
SB;
PRIO(0);
const u32x4 w168 = RDW(0, 57344);
MMF(a2_0, 224, 227, w164, x9_0);
PREFILL(1024);
MMF(a2_1, 228, 231, w164, x9_1);
SB;
const u32x4 w169 = RDW(0, 58368);
MMF(a3_0, 232, 235, w165, x9_0);
MMF(a3_1, 236, 239, w165, x9_1);
SB;
SYNC(5);
PRIO(3);
const u32x4 w170 = RDW(0, 59392);
MMF(a4_0, 240, 243, w166, x9_0);
const f32x4 cc1_4 = RDCS(640);
FLAG(5, 1, sWw);
MMF(a4_1, 244, 247, w166, x9_1);
SB;
const u32x4 w171 = RDW(0, 60416);
MMF(a5_0, 248, 251, w167, x9_0);
MMF(a5_1, 252, 255, w167, x9_1);
MM(gram, x10_0, x10_0);
SB;
const u32x4 w172 = RDW(0, 61440);
MMF(a0_0, 208, 211, w168, x10_0);
DMA(sW1, 253952, vL16, 114688);
MMF(a0_1, 212, 215, w168, x10_1);
SB;
const u32x4 w173 = RDW(0, 62464);
MMF(a1_0, 216, 219, w169, x10_0);
CORR(a4_0, 240, 241, 242, 243, cc1_4, 0);
MMF(a1_1, 220, 223, w169, x10_1);
SB;
const u32x4 w174 = RDW(0, 63488);
MMF(a2_0, 224, 227, w170, x10_0);
CORR(a4_1, 244, 245, 246, 247, cc1_4, 1);
MMF(a2_1, 228, 231, w170, x10_1);
SB;
const u32x4 w175 = RDW(0, 64512);
MMF(a3_0, 232, 235, w171, x10_0);
MMF(a3_1, 236, 239, w171, x10_1);
SB;
const u32x4 w176 = RDW(2, 0);
MMF(a4_0, 240, 243, w172, x10_0);
PREFILL(2048);
MMF(a4_1, 244, 247, w172, x10_1);
MM(gram, x11_0, x11_0);
SB;
PRIO(2);
const u32x4 w177 = RDW(2, 1024);
MMF(a5_0, 248, 251, w173, x10_0);
const f32x4 cc1_5 = RDCS(896);
MMF(a5_1, 252, 255, w173, x10_1);
SB;
const u32x4 w178 = RDW(2, 2048);
MMF(a0_0, 208, 211, w174, x11_0);
MMF(a0_1, 212, 215, w174, x11_1);
SB;
const u32x4 w179 = RDW(2, 3072);
MMF(a1_0, 216, 219, w175, x11_0);
MMF(a1_1, 220, 223, w175, x11_1);
SB;
const u32x4 w180 = RDW(2, 4096);
MMF(a2_0, 224, 227, w176, x11_0);
CORR(a5_0, 248, 249, 250, 251, cc1_5, 0);
DMA(sW1, 262144, vL16, 32768);
MMF(a2_1, 228, 231, w176, x11_1);
SB;
const u32x4 w181 = RDW(2, 5120);
MMF(a3_0, 232, 235, w177, x11_0);
CORR(a5_1, 252, 253, 254, 255, cc1_5, 1);
MMF(a3_1, 236, 239, w177, x11_1);
MM(gram, x12_0, x12_0);
SB;
const u32x4 w182 = RDW(2, 6144);
MMF(a4_0, 240, 243, w178, x11_0);
MMF(a4_1, 244, 247, w178, x11_1);
SB;
const u32x4 w183 = RDW(2, 7168);
MMF(a5_0, 248, 251, w179, x11_0);
POLL(sFr, vL16, 8);
MMF(a5_1, 252, 255, w179, x11_1);
SB;
const u32x4 w184 = RDW(1, 0);
MMF(a0_0, 208, 211, w180, x12_0);
PREFILL(3072);
MMF(a0_1, 212, 215, w180, x12_1);
SB;
PRIO(1);
const u32x4 w185 = RDW(1, 1024);
MMF(a1_0, 216, 219, w181, x12_0);
MMF(a1_1, 220, 223, w181, x12_1);
SB;
const u32x4 w186 = RDW(1, 2048);
MMF(a2_0, 224, 227, w182, x12_0);
MMF(a2_1, 228, 231, w182, x12_1);
MM(gram, x13_0, x13_0);
SB;
const u32x4 w187 = RDW(1, 3072);
MMF(a3_0, 232, 235, w183, x12_0);
MMF(a3_1, 236, 239, w183, x12_1);
SB;
const u32x4 w188 = RDW(1, 4096);
MMF(a4_0, 240, 243, w184, x12_0);
DMA(sW1, 270336, vL16, 40960);
MMF(a4_1, 244, 247, w184, x12_1);
SB;
const u32x4 w189 = RDW(1, 5120);
MMF(a5_0, 248, 251, w185, x12_0);
MMF(a5_1, 252, 255, w185, x12_1);
SB;
const u32x4 w190 = RDW(1, 6144);
MMF(a0_0, 208, 211, w186, x13_0);
MMF(a0_1, 212, 215, w186, x13_1);
SB;
const u32x4 w191 = RDW(1, 7168);
MMF(a1_0, 216, 219, w187, x13_0);
MMF(a1_1, 220, 223, w187, x13_1);
MM(gram, x14_0, x14_0);
SB;
const u32x4 w192 = RDW(1, 8192);
MMF(a2_0, 224, 227, w188, x13_0);
PREFILL(4096);
MMF(a2_1, 228, 231, w188, x13_1);
SB;
PRIO(0);
const u32x4 w193 = RDW(1, 9216);
MMF(a3_0, 232, 235, w189, x13_0);
CMP(3, 1);
MMF(a3_1, 236, 239, w189, x13_1);
SB;
const u32x4 w194 = RDW(1, 10240);
MMF(a4_0, 240, 243, w190, x13_0);
MMF(a4_1, 244, 247, w190, x13_1);
SB;
SYNC(9);
PRIO(3);
const u32x4 w195 = RDW(1, 11264);
MMF(a5_0, 248, 251, w191, x13_0);
CHECK(1, sFr, vL16, 8);
MMF(a5_1, 252, 255, w191, x13_1);
SB;
const u32x4 w196 = RDW(1, 12288);
MMF(a0_0, 208, 211, w192, x14_0);
DMA(sW1, 278528, vL16, 122880);
MMF(a0_1, 212, 215, w192, x14_1);
MM(gram, x15_0, x15_0);
SB;
const u32x4 w197 = RDW(1, 13312);
MMF(a1_0, 216, 219, w193, x14_0);
MMF(a1_1, 220, 223, w193, x14_1);
SB;
const u32x4 w198 = RDW(1, 14336);
MMF(a2_0, 224, 227, w194, x14_0);
RES_SRC();
MMF(a2_1, 228, 231, w194, x14_1);
SB;
const u32x4 w199 = RDW(1, 15360);
MMF(a3_0, 232, 235, w195, x14_0);
MMF(a3_1, 236, 239, w195, x14_1);
SB;
const u32x4 w200 = RDW(2, 8192);
MMF(a4_0, 240, 243, w196, x14_0);
DMA(sR, 0, vR, 0);
MMF(a4_1, 244, 247, w196, x14_1);
SB;
const u32x4 w201 = RDW(2, 9216);
MMF(a5_0, 248, 251, w197, x14_0);
MMF(a5_1, 252, 255, w197, x14_1);
MM(gram, x16_0, x16_0);
SB;
const u32x4 w202 = RDW(2, 10240);
MMF(a0_0, 208, 211, w198, x15_0);
RES_LD8(8192);
MMF(a0_1, 212, 215, w198, x15_1);
SB;
const u32x4 w203 = RDW(2, 11264);
MMF(a1_0, 216, 219, w199, x15_0);
MMF(a1_1, 220, 223, w199, x15_1);
SB;
const u32x4 w204 = RDW(2, 12288);
MMF(a2_0, 224, 227, w200, x15_0);
MMF(a2_1, 228, 231, w200, x15_1);
SB;
const u32x4 w205 = RDW(2, 13312);
MMF(a3_0, 232, 235, w201, x15_0);
MMF(a3_1, 236, 239, w201, x15_1);
SB;
const u32x4 w206 = RDW(2, 14336);
MMF(a4_0, 240, 243, w202, x15_0);
DMA(sW1, 286720, vL16, 16384);
MMF(a4_1, 244, 247, w202, x15_1);
MM(gram, x17_0, x17_0);
SB;
const u32x4 w207 = RDW(2, 15360);
MMF(a5_0, 248, 251, w203, x15_0);
MMF(a5_1, 252, 255, w203, x15_1);
SB;
const u32x4 w208 = RDW(1, 16384);
MMF(a0_0, 208, 211, w204, x16_0);
RS_ADR();
MMF(a0_1, 212, 215, w204, x16_1);
SB;
const u32x4 w209 = RDW(1, 17408);
MMF(a1_0, 216, 219, w205, x16_0);
MMF(a1_1, 220, 223, w205, x16_1);
SB;
const u32x4 w210 = RDW(1, 18432);
MMF(a2_0, 224, 227, w206, x16_0);
RS_FIN();
DMA(sW1, 294912, vL16, 24576);
MMF(a2_1, 228, 231, w206, x16_1);
SB;
const u32x4 w211 = RDW(1, 19456);
MMF(a3_0, 232, 235, w207, x16_0);
MMF(a3_1, 236, 239, w207, x16_1);
SB;
const u32x4 w212 = RDW(1, 20480);
MMF(a4_0, 240, 243, w208, x16_0);
MMF(a4_1, 244, 247, w208, x16_1);
SB;
const u32x4 w213 = RDW(1, 21504);
MMF(a5_0, 248, 251, w209, x16_0);
MMF(a5_1, 252, 255, w209, x16_1);
SB;
const u32x4 w214 = RDW(1, 22528);
MMF(a0_0, 208, 211, w210, x17_0);
RES_RD(2, 0, 8192);
DMA(sW1, 303104, vL16, 49152);
MMF(a0_1, 212, 215, w210, x17_1);
SB;
const u32x4 w215 = RDW(1, 23552);
MMF(a1_0, 216, 219, w211, x17_0);
MMF(a1_1, 220, 223, w211, x17_1);
SB;
const u32x4 w216 = RDW(1, 24576);
MMF(a2_0, 224, 227, w212, x17_0);
FLAG(6, 1, sIw);
MMF(a2_1, 228, 231, w212, x17_1);
SB;
const u32x4 w217 = RDW(1, 25600);
MMF(a3_0, 232, 235, w213, x17_0);

PIN2(res, res8);
MMF(a3_1, 236, 239, w213, x17_1);
SB;
const u32x4 w218 = RDW(1, 26624);
MMF(a4_0, 240, 243, w214, x17_0);
DMA(sW1, 311296, vL16, 57344);
MMF(a4_1, 244, 247, w214, x17_1);
SB;
const u32x4 w219 = RDW(1, 27648);
MMF(a5_0, 248, 251, w215, x17_0);

MMF(a5_1, 252, 255, w215, x17_1);
SB; PRIO(0);
GATE(p3_0, a0_0, 208, 209, 210, 211, a1_0);
GATE(p3_1, a0_1, 212, 213, 214, 215, a1_1); SB;

GATE(p4_0, a2_0, 224, 225, 226, 227, a3_0);
GATE(p4_1, a2_1, 228, 229, 230, 231, a3_1); SB;
GATE(p5_0, a4_0, 240, 241, 242, 243, a5_0);
GATE(p5_1, a4_1, 244, 245, 246, 247, a5_1); SB;
SB;

PRIO(2);
const u32x4 w220 = RDW(1, 28672);
MMF0(a0_0, 208, 211, w216, x0_0);
HST4(p2_0, p3_0, 0, 1024);
MMF0(a0_1, 212, 215, w216, x0_1);
SB;
const u32x4 w221 = RDW(1, 29696);
MMF0(a1_0, 216, 219, w217, x0_0);
MMF0(a1_1, 220, 223, w217, x0_1);
SB;
const u32x4 w222 = RDW(1, 30720);
MMF0(a2_0, 224, 227, w218, x0_0);
MMF0(a2_1, 228, 231, w218, x0_1);
SB;
const u32x4 w223 = RDW(1, 31744);
MMF0(a3_0, 232, 235, w219, x0_0);
DMA(sW1, 319488, vL16, 131072);
MMF0(a3_1, 236, 239, w219, x0_1);
SB;
const u32x4 w224 = RDW(2, 16384);
MMF0(a4_0, 240, 243, w220, x0_0);
MMF0(a4_1, 244, 247, w220, x0_1);
SB;
const u32x4 w225 = RDW(2, 17408);
MMF0(a5_0, 248, 251, w221, x0_0);
MMF0(a5_1, 252, 255, w221, x0_1);
SB;
const u32x4 w226 = RDW(2, 18432);
MMF(a0_0, 208, 211, w222, x1_0);
u32x4 h0_0; HLD4(h0_0, 0, 0);
MMF(a0_1, 212, 215, w222, x1_1);
SB;
const u32x4 w227 = RDW(2, 19456);
MMF(a1_0, 216, 219, w223, x1_0);
MMF(a1_1, 220, 223, w223, x1_1);
SB;
const u32x4 w228 = RDW(2, 20480);
MMF(a2_0, 224, 227, w224, x1_0);
HST4(p2_1, p3_1, 1, 1024);
MMF(a2_1, 228, 231, w224, x1_1);
SB;
const u32x4 w229 = RDW(2, 21504);
MMF(a3_0, 232, 235, w225, x1_0);
MMF(a3_1, 236, 239, w225, x1_1);
SB;
const u32x4 w230 = RDW(2, 22528);
MMF(a4_0, 240, 243, w226, x1_0);
POLL(sWr, vL16, 6);
MMF(a4_1, 244, 247, w226, x1_1);
SB;
PRIO(1);
const u32x4 w231 = RDW(2, 23552);
MMF(a5_0, 248, 251, w227, x1_0);
u32x4 h0_1; HLD4(h0_1, 1, 0);
MMF(a5_1, 252, 255, w227, x1_1);
SB;
const u32x4 w232 = RDW(1, 32768);
MMF(a0_0, 208, 211, w228, x2_0);
MMF(a0_1, 212, 215, w228, x2_1);
SB;
const u32x4 w233 = RDW(1, 33792);
MMF(a1_0, 216, 219, w229, x2_0);
MMF(a1_1, 220, 223, w229, x2_1);
SB;
const u32x4 w234 = RDW(1, 34816);
MMF(a2_0, 224, 227, w230, x2_0);
u32x4 h1_0; HLD4(h1_0, 0, 4096);
MMF(a2_1, 228, 231, w230, x2_1);
SB;
const u32x4 w235 = RDW(1, 35840);
MMF(a3_0, 232, 235, w231, x2_0);
MMF(a3_1, 236, 239, w231, x2_1);
SB;
const u32x4 w236 = RDW(1, 36864);
MMF(a4_0, 240, 243, w232, x2_0);
HST4(p4_0, p5_0, 0, 2048);
MMF(a4_1, 244, 247, w232, x2_1);
SB;
const u32x4 w237 = RDW(1, 37888);
MMF(a5_0, 248, 251, w233, x2_0);
MMF(a5_1, 252, 255, w233, x2_1);
SB;
const u32x4 w238 = RDW(1, 38912);
MMF(a0_0, 208, 211, w234, x3_0);
MMF(a0_1, 212, 215, w234, x3_1);
SB;
const u32x4 w239 = RDW(1, 39936);
MMF(a1_0, 216, 219, w235, x3_0);
u32x4 h1_1; HLD4(h1_1, 1, 4096);
MMF(a1_1, 220, 223, w235, x3_1);
SB;
const u32x4 w240 = RDW(1, 40960);
MMF(a2_0, 224, 227, w236, x3_0);
CMP(4, 1);
MMF(a2_1, 228, 231, w236, x3_1);
SB;
const u32x4 w241 = RDW(1, 41984);
MMF(a3_0, 232, 235, w237, x3_0);
MMF(a3_1, 236, 239, w237, x3_1);
SB;
PRIO(0);
const u32x4 w242 = RDW(1, 43008);
MMF(a4_0, 240, 243, w238, x3_0);
CHECK(1, sWr, vL16, 6);
u32x4 h2_0; HLD4(h2_0, 0, 9216);
MMF(a4_1, 244, 247, w238, x3_1);
SB;
const u32x4 w243 = RDW(1, 44032);
MMF(a5_0, 248, 251, w239, x3_0);
MMF(a5_1, 252, 255, w239, x3_1);
SB;
SYNC(19);
PRIO(3);
const u32x4 w244 = RDW(1, 45056);
MMF(a0_0, 208, 211, w240, x4_0);
HST4(p4_1, p5_1, 1, 2048);
MMF(a0_1, 212, 215, w240, x4_1);
SB;
const u32x4 w245 = RDW(1, 46080);
MMF(a1_0, 216, 219, w241, x4_0);
MMF(a1_1, 220, 223, w241, x4_1);
SB;
const u32x4 w246 = RDW(1, 47104);
MMF(a2_0, 224, 227, w242, x4_0);
MMF(a2_1, 228, 231, w242, x4_1);
SB;
const u32x4 w247 = RDW(1, 48128);
MMF(a3_0, 232, 235, w243, x4_0);
DMA(sWm, 0, vL16, 65536);
MMF(a3_1, 236, 239, w243, x4_1);
SB;
const u32x4 w248 = RDW(1, 49152);
MMF(a4_0, 240, 243, w244, x4_0);
MMF(a4_1, 244, 247, w244, x4_1);
SB;
const u32x4 w249 = RDW(1, 50176);
MMF(a5_0, 248, 251, w245, x4_0);
CS_BASE();
MMF(a5_1, 252, 255, w245, x4_1);
SB;
const u32x4 w250 = RDW(1, 51200);
MMF(a0_0, 208, 211, w246, x5_0);
const f32x4 cc2_0 = RDCS(1024);
DMA(sWd, 8192, vL16, 73728);
MMF(a0_1, 212, 215, w246, x5_1);
SB;
PRIO(2);
const u32x4 w251 = RDW(1, 52224);
MMF(a1_0, 216, 219, w247, x5_0);
MMF(a1_1, 220, 223, w247, x5_1);
SB;
const u32x4 w252 = RDW(1, 53248);
MMF(a2_0, 224, 227, w248, x5_0);
MMF(a2_1, 228, 231, w248, x5_1);
SB;
const u32x4 w253 = RDW(1, 54272);
MMF(a3_0, 232, 235, w249, x5_0);
CORR(a0_0, 208, 209, 210, 211, cc2_0, 0);
DMA(sWd, 16384, vL16, 139264);
MMF(a3_1, 236, 239, w249, x5_1);
SB;
const u32x4 w254 = RDW(1, 55296);
MMF(a4_0, 240, 243, w250, x5_0);
CORR(a0_1, 212, 213, 214, 215, cc2_0, 1);
MMF(a4_1, 244, 247, w250, x5_1);
SB;
const u32x4 w255 = RDW(1, 56320);
MMF(a5_0, 248, 251, w251, x5_0);
MMF(a5_1, 252, 255, w251, x5_1);
SB;
const u32x4 w256 = RDW(0, 32768);
MMF(a0_0, 208, 211, w252, x6_0);
DMA(sWd, 24576, vL16, 81920);
MMF(a0_1, 212, 215, w252, x6_1);
SB;
const u32x4 w257 = RDW(0, 33792);
MMF(a1_0, 216, 219, w253, x6_0);
const f32x4 cc2_1 = RDCS(1280);
MMF(a1_1, 220, 223, w253, x6_1);
SB;
PRIO(1);
const u32x4 w258 = RDW(0, 34816);
MMF(a2_0, 224, 227, w254, x6_0);
MMF(a2_1, 228, 231, w254, x6_1);
SB;
const u32x4 w259 = RDW(0, 35840);
MMF(a3_0, 232, 235, w255, x6_0);
DMA(sWd, 32768, vL16, 90112);
MMF(a3_1, 236, 239, w255, x6_1);
SB;
const u32x4 w260 = RDW(0, 36864);
MMF(a4_0, 240, 243, w256, x6_0);
CORR(a1_0, 216, 217, 218, 219, cc2_1, 0);
MMF(a4_1, 244, 247, w256, x6_1);
SB;
const u32x4 w261 = RDW(0, 37888);
MMF(a5_0, 248, 251, w257, x6_0);
CORR(a1_1, 220, 221, 222, 223, cc2_1, 1);
MMF(a5_1, 252, 255, w257, x6_1);
SB;
const u32x4 w262 = RDW(0, 38912);
MMF(a0_0, 208, 211, w258, x7_0);
DMA(sWd, 40960, vL16, 147456);
MMF(a0_1, 212, 215, w258, x7_1);
SB;
const u32x4 w263 = RDW(0, 39936);
MMF(a1_0, 216, 219, w259, x7_0);
FLAG(6, 2, sFw);
MMF(a1_1, 220, 223, w259, x7_1);
SB;
const u32x4 w264 = RDW(0, 40960);
MMF(a2_0, 224, 227, w260, x7_0);
const f32x4 cc2_2 = RDCS(1088);
MMF(a2_1, 228, 231, w260, x7_1);
SB;
const u32x4 w265 = RDW(0, 41984);
MMF(a3_0, 232, 235, w261, x7_0);
DMA(sWd, 49152, vL16, 98304);
MMF(a3_1, 236, 239, w261, x7_1);
SB;
PRIO(0);
const u32x4 w266 = RDW(0, 43008);
MMF(a4_0, 240, 243, w262, x7_0);
MMF(a4_1, 244, 247, w262, x7_1);
SB;
const u32x4 w267 = RDW(0, 44032);
MMF(a5_0, 248, 251, w263, x7_0);
CORR(a2_0, 224, 225, 226, 227, cc2_2, 0);
MMF(a5_1, 252, 255, w263, x7_1);
SB;
SYNC(22);
PRIO(3);
const u32x4 w268 = RDW(0, 45056);
MMF(a0_0, 208, 211, w264, x8_0);
CORR(a2_1, 228, 229, 230, 231, cc2_2, 1);
DMA(sWd, 57344, vL16, 0);
MMF(a0_1, 212, 215, w264, x8_1);
SB;
const u32x4 w269 = RDW(0, 46080);
MMF(a1_0, 216, 219, w265, x8_0);
MMF(a1_1, 220, 223, w265, x8_1);
SB;
const u32x4 w270 = RDW(0, 47104);
MMF(a2_0, 224, 227, w266, x8_0);
MMF(a2_1, 228, 231, w266, x8_1);
SB;
const u32x4 w271 = RDW(0, 48128);
MMF(a3_0, 232, 235, w267, x8_0);
const f32x4 cc2_3 = RDCS(1344);
u32x4 h2_1; HLD4(h2_1, 1, 9216);
MMF(a3_1, 236, 239, w267, x8_1);
SB;
const u32x4 w272 = RDW(1, 57344);
MMF(a4_0, 240, 243, w268, x8_0);
MMF(a4_1, 244, 247, w268, x8_1);
SB;
const u32x4 w273 = RDW(1, 58368);
MMF(a5_0, 248, 251, w269, x8_0);
MMF(a5_1, 252, 255, w269, x8_1);
SB;
const u32x4 w274 = RDW(1, 59392);
MMF(a0_0, 208, 211, w270, x9_0);
CORR(a3_0, 232, 233, 234, 235, cc2_3, 0);
u32x4 h3_0; HLD4(h3_0, 0, 13312);
MMF(a0_1, 212, 215, w270, x9_1);
SB;
PRIO(2);
const u32x4 w275 = RDW(1, 60416);
MMF(a1_0, 216, 219, w271, x9_0);
CORR(a3_1, 236, 237, 238, 239, cc2_3, 1);
MMF(a1_1, 220, 223, w271, x9_1);
SB;
const u32x4 w276 = RDW(1, 61440);
MMF(a2_0, 224, 227, w272, x9_0);
MMF(a2_1, 228, 231, w272, x9_1);
SB;
const u32x4 w277 = RDW(1, 62464);
MMF(a3_0, 232, 235, w273, x9_0);
u32x4 h3_1; HLD4(h3_1, 1, 13312);
MMF(a3_1, 236, 239, w273, x9_1);
SB;
const u32x4 w278 = RDW(1, 63488);
MMF(a4_0, 240, 243, w274, x9_0);
const f32x4 cc2_4 = RDCS(1152);
MMF(a4_1, 244, 247, w274, x9_1);
SB;
const u32x4 w279 = RDW(1, 64512);
MMF(a5_0, 248, 251, w275, x9_0);
MMF(a5_1, 252, 255, w275, x9_1);
SB;
const u32x4 w280 = RDW(0, 16384);
MMF(a0_0, 208, 211, w276, x10_0);
DMA(sWd, 65536, vL16, 8192);
MMF(a0_1, 212, 215, w276, x10_1);
SB;
const u32x4 w281 = RDW(0, 17408);
MMF(a1_0, 216, 219, w277, x10_0);
CORR(a4_0, 240, 241, 242, 243, cc2_4, 0);
MMF(a1_1, 220, 223, w277, x10_1);
SB;
PRIO(1);
const u32x4 w282 = RDW(0, 18432);
MMF(a2_0, 224, 227, w278, x10_0);
CORR(a4_1, 244, 245, 246, 247, cc2_4, 1);
MMF(a2_1, 228, 231, w278, x10_1);
SB;
const u32x4 w283 = RDW(0, 19456);
MMF(a3_0, 232, 235, w279, x10_0);
POLL(sFr, vL16, 8);
DMA(sWd, 73728, vL16, 106496);
MMF(a3_1, 236, 239, w279, x10_1);
SB;
const u32x4 w284 = RDW(0, 20480);
MMF(a4_0, 240, 243, w280, x10_0);
MMF(a4_1, 244, 247, w280, x10_1);
SB;
const u32x4 w285 = RDW(0, 21504);
MMF(a5_0, 248, 251, w281, x10_0);
const f32x4 cc2_5 = RDCS(1408);
MMF(a5_1, 252, 255, w281, x10_1);
SB;
const u32x4 w286 = RDW(0, 22528);
MMF(a0_0, 208, 211, w282, x11_0);
DMA(sWd, 81920, vL16, 114688);
MMF(a0_1, 212, 215, w282, x11_1);
SB;
const u32x4 w287 = RDW(0, 23552);
MMF(a1_0, 216, 219, w283, x11_0);
MMF(a1_1, 220, 223, w283, x11_1);
SB;
const u32x4 w288 = RDW(0, 24576);
MMF(a2_0, 224, 227, w284, x11_0);
CORR(a5_0, 248, 249, 250, 251, cc2_5, 0);
MMF(a2_1, 228, 231, w284, x11_1);
SB;
const u32x4 w289 = RDW(0, 25600);
MMF(a3_0, 232, 235, w285, x11_0);
CORR(a5_1, 252, 253, 254, 255, cc2_5, 1);
DMA(sWd, 90112, vL16, 32768);
MMF(a3_1, 236, 239, w285, x11_1);
SB;
PRIO(0);
const u32x4 w290 = RDW(0, 26624);
MMF(a4_0, 240, 243, w286, x11_0);
MMF(a4_1, 244, 247, w286, x11_1);
SB;
const u32x4 w291 = RDW(0, 27648);
MMF(a5_0, 248, 251, w287, x11_0);
CMP(3, 2);
MMF(a5_1, 252, 255, w287, x11_1);
SB;
SYNC(30);
PRIO(3);
const u32x4 w292 = RDW(0, 28672);
MMF(a0_0, 208, 211, w288, x12_0);
DMA(sWd, 98304, vL16, 40960);
MMF(a0_1, 212, 215, w288, x12_1);
SB;
const u32x4 w293 = RDW(0, 29696);
MMF(a1_0, 216, 219, w289, x12_0);
CHECK(2, sFr, vL16, 8);
MMF(a1_1, 220, 223, w289, x12_1);
SB;
const u32x4 w294 = RDW(0, 30720);
MMF(a2_0, 224, 227, w290, x12_0);
MMF(a2_1, 228, 231, w290, x12_1);
SB;
PRIO(2);
const u32x4 w295 = RDW(0, 31744);
MMF(a3_0, 232, 235, w291, x12_0);
u32x4 h4_0; HLD4(h4_0, 0, 1024);
MMF(a3_1, 236, 239, w291, x12_1);
SB;
const u32x4 w296 = RDW(0, 49152);
MMF(a4_0, 240, 243, w292, x12_0);
MMF(a4_1, 244, 247, w292, x12_1);
SB;
const u32x4 w297 = RDW(0, 50176);
MMF(a5_0, 248, 251, w293, x12_0);
MMF(a5_1, 252, 255, w293, x12_1);
SB;
PRIO(1);
const u32x4 w298 = RDW(0, 51200);
MMF(a0_0, 208, 211, w294, x13_0);
u32x4 h4_1; HLD4(h4_1, 1, 1024);
MMF(a0_1, 212, 215, w294, x13_1);
SB;
const u32x4 w299 = RDW(0, 52224);
MMF(a1_0, 216, 219, w295, x13_0);
MMF(a1_1, 220, 223, w295, x13_1);
SB;
const u32x4 w300 = RDW(0, 53248);
MMF(a2_0, 224, 227, w296, x13_0);
POLL(sIr, vL16, 4);
MMF(a2_1, 228, 231, w296, x13_1);
SB;
PRIO(0);
const u32x4 w301 = RDW(0, 54272);
MMF(a3_0, 232, 235, w297, x13_0);
u32x4 h5_0; HLD4(h5_0, 0, 5120);
MMF(a3_1, 236, 239, w297, x13_1);
SB;
const u32x4 w302 = RDW(0, 55296);
MMF(a4_0, 240, 243, w298, x13_0);
MMF(a4_1, 244, 247, w298, x13_1);
SB;
SYNC(13);
PRIO(3);
const u32x4 w303 = RDW(0, 56320);
MMF(a5_0, 248, 251, w299, x13_0);
MMF(a5_1, 252, 255, w299, x13_1);
SB;
const u32x4 w304 = RDW(0, 57344);
MMF(a0_0, 208, 211, w300, x14_0);
u32x4 h5_1; HLD4(h5_1, 1, 5120);
MMF(a0_1, 212, 215, w300, x14_1);
SB;
const u32x4 w305 = RDW(0, 58368);
MMF(a1_0, 216, 219, w301, x14_0);
MMF(a1_1, 220, 223, w301, x14_1);
SB;
const u32x4 w306 = RDW(0, 59392);
MMF(a2_0, 224, 227, w302, x14_0);
MMF(a2_1, 228, 231, w302, x14_1);
SB;
const u32x4 w307 = RDW(0, 60416);
MMF(a3_0, 232, 235, w303, x14_0);
u32x4 h6_0; HLD4(h6_0, 0, 10240);
MMF(a3_1, 236, 239, w303, x14_1);
SB;
const u32x4 w308 = RDW(0, 61440);
MMF(a4_0, 240, 243, w304, x14_0);
MMF(a4_1, 244, 247, w304, x14_1);
SB;
const u32x4 w309 = RDW(0, 62464);
MMF(a5_0, 248, 251, w305, x14_0);
MMF(a5_1, 252, 255, w305, x14_1);
SB;
const u32x4 w310 = RDW(0, 63488);
MMF(a0_0, 208, 211, w306, x15_0);
CMP(3, 1);
u32x4 h6_1; HLD4(h6_1, 1, 10240);
MMF(a0_1, 212, 215, w306, x15_1);
SB;
const u32x4 w311 = RDW(0, 64512);
MMF(a1_0, 216, 219, w307, x15_0);
MMF(a1_1, 220, 223, w307, x15_1);
SB;
const u32x4 w312 = RDW(2, 0);
MMF(a2_0, 224, 227, w308, x15_0);
CHECK(1, sIr, vL16, 4);
MMF(a2_1, 228, 231, w308, x15_1);
SB;
const u32x4 w313 = RDW(2, 1024);
MMF(a3_0, 232, 235, w309, x15_0);
DMA(sWd, 106496, vL16, 122880);
MMF(a3_1, 236, 239, w309, x15_1);
SB;
const u32x4 w314 = RDW(2, 2048);
MMF(a4_0, 240, 243, w310, x15_0);
MMF(a4_1, 244, 247, w310, x15_1);
SB;
const u32x4 w315 = RDW(2, 3072);
MMF(a5_0, 248, 251, w311, x15_0);
MMF(a5_1, 252, 255, w311, x15_1);
SB;
const u32x4 w316 = RDW(2, 4096);
MMF(a0_0, 208, 211, w312, x16_0);
DMA(sWd, 114688, vL16, 16384);
MMF(a0_1, 212, 215, w312, x16_1);
SB;
const u32x4 w317 = RDW(2, 5120);
MMF(a1_0, 216, 219, w313, x16_0);
MMF(a1_1, 220, 223, w313, x16_1);
SB;
const u32x4 w318 = RDW(2, 6144);
MMF(a2_0, 224, 227, w314, x16_0);
MMF(a2_1, 228, 231, w314, x16_1);
SB;
const u32x4 w319 = RDW(2, 7168);
MMF(a3_0, 232, 235, w315, x16_0);
DMA(sWd, 122880, vL16, 24576);
MMF(a3_1, 236, 239, w315, x16_1);
SB;
const u32x4 w320 = RDW(1, 0);
MMF(a4_0, 240, 243, w316, x16_0);
MMF(a4_1, 244, 247, w316, x16_1);
SB;
const u32x4 w321 = RDW(1, 1024);
MMF(a5_0, 248, 251, w317, x16_0);
MMF(a5_1, 252, 255, w317, x16_1);
SB;
const u32x4 w322 = RDW(1, 2048);
MMF(a0_0, 208, 211, w318, x17_0);
u32x4 h7_0; HLD4(h7_0, 0, 14336);
MMF(a0_1, 212, 215, w318, x17_1);
SB;
const u32x4 w323 = RDW(1, 3072);
MMF(a1_0, 216, 219, w319, x17_0);
MMF(a1_1, 220, 223, w319, x17_1);
SB;
G2_BASES();
const u32x4 w324 = RDW(1, 4096);
MMF(a2_0, 224, 227, w320, x17_0);
MMF(a2_1, 228, 231, w320, x17_1);
SB;
const u32x4 w325 = RDW(1, 6144);
MMF(a3_0, 232, 235, w321, x17_0);

u32x4 h7_1; HLD4(h7_1, 1, 14336);
MMF(a3_1, 236, 239, w321, x17_1);
SB;
const u32x4 w326 = RDW(1, 8192);
MMF(a4_0, 240, 243, w322, x17_0);
MMF(a4_1, 244, 247, w322, x17_1);
SB;
const u32x4 w327 = RDW(1, 10240);
MMF(a5_0, 248, 251, w323, x17_0);

MMF(a5_1, 252, 255, w323, x17_1);
SB; PRIO(0);
GATE(p6_0, a0_0, 208, 209, 210, 211, a1_0);
GATE(p6_1, a0_1, 212, 213, 214, 215, a1_1); SB;

GATE(p7_0, a2_0, 224, 225, 226, 227, a3_0);
GATE(p7_1, a2_1, 228, 229, 230, 231, a3_1); SB;
GATE(p8_0, a4_0, 240, 241, 242, 243, a5_0);
GATE(p8_1, a4_1, 244, 245, 246, 247, a5_1); SB;
SB;

PRIO(2);
const u32x4 w328 = RDW(1, 12288);
HWAIT4(35, h0_0, h0_1);
f32x4 o0_0; MMF0(o0_0, 184, 187, w324, h0_0);
HST4(p6_0, p7_0, 0, 3072);
f32x4 o0_1; MMF0(o0_1, 188, 191, w324, h0_1);
SB;
const u32x4 w329 = RDW(1, 14336);
f32x4 o1_0; MMF0(o1_0, 192, 195, w325, h0_0);
f32x4 o1_1; MMF0(o1_1, 196, 199, w325, h0_1);
SB;
const u32x4 w330 = RDW(2, 8192);
f32x4 o2_0; MMF0(o2_0, 200, 203, w326, h0_0);
HST4(p6_1, p7_1, 1, 3072);
f32x4 o2_1; MMF0(o2_1, 204, 207, w326, h0_1);
SB;
const u32x4 w331 = RDW(2, 10240);
f32x4 o3_0; MMF0(o3_0, 208, 211, w327, h0_0);
f32x4 o3_1; MMF0(o3_1, 212, 215, w327, h0_1);
SB;
const u32x4 w332 = RDW(2, 12288);
f32x4 o4_0; MMF0(o4_0, 216, 219, w328, h0_0);
HST2(p8_0, 0);
f32x4 o4_1; MMF0(o4_1, 220, 223, w328, h0_1);
SB;
const u32x4 w333 = RDW(2, 14336);
f32x4 o5_0; MMF0(o5_0, 224, 227, w329, h0_0);
f32x4 o5_1; MMF0(o5_1, 228, 231, w329, h0_1);
SB;
const u32x4 w334 = RDW(1, 16384);
f32x4 o8_0; MMF0(o8_0, 232, 235, w330, h0_0);
HST2(p8_1, 1);
f32x4 o8_1; MMF0(o8_1, 236, 239, w330, h0_1);
SB;
const u32x4 w335 = RDW(1, 18432);
HWAIT4(36, h1_0, h1_1);
MMF(o0_0, 184, 187, w331, h1_0);
MMF(o0_1, 188, 191, w331, h1_1);
SB;
const u32x4 w336 = RDW(1, 20480);
MMF(o1_0, 192, 195, w332, h1_0);
MMF(o1_1, 196, 199, w332, h1_1);
SB;
const u32x4 w337 = RDW(1, 22528);
MMF(o2_0, 200, 203, w333, h1_0);
MMF(o2_1, 204, 207, w333, h1_1);
SB;
const u32x4 w338 = RDW(1, 24576);
MMF(o3_0, 208, 211, w334, h1_0);
MMF(o3_1, 212, 215, w334, h1_1);
SB;
PRIO(1);
const u32x4 w339 = RDW(1, 26624);
MMF(o4_0, 216, 219, w335, h1_0);
MMF(o4_1, 220, 223, w335, h1_1);
SB;
const u32x4 w340 = RDW(1, 28672);
MMF(o5_0, 224, 227, w336, h1_0);
MMF(o5_1, 228, 231, w336, h1_1);
SB;
const u32x4 w341 = RDW(1, 30720);
MMF(o8_0, 232, 235, w337, h1_0);
MMF(o8_1, 236, 239, w337, h1_1);
SB;
const u32x4 w342 = RDW(2, 16384);
HWAIT4(24, h2_0, h2_1);
MMF(o0_0, 184, 187, w338, h2_0);
MMF(o0_1, 188, 191, w338, h2_1);
SB;
const u32x4 w343 = RDW(2, 18432);
MMF(o1_0, 192, 195, w339, h2_0);
MMF(o1_1, 196, 199, w339, h2_1);
SB;
const u32x4 w344 = RDW(2, 20480);
MMF(o2_0, 200, 203, w340, h2_0);
u32x4 h8_0; HLD4(h8_0, 0, 2048);
MMF(o2_1, 204, 207, w340, h2_1);
SB;
const u32x4 w345 = RDW(2, 22528);
MMF(o3_0, 208, 211, w341, h2_0);
MMF(o3_1, 212, 215, w341, h2_1);
SB;
const u32x4 w346 = RDW(1, 32768);
MMF(o4_0, 216, 219, w342, h2_0);
u32x4 h8_1; HLD4(h8_1, 1, 2048);
MMF(o4_1, 220, 223, w342, h2_1);
SB;
const u32x4 w347 = RDW(1, 34816);
MMF(o5_0, 224, 227, w343, h2_0);
MMF(o5_1, 228, 231, w343, h2_1);
SB;
const u32x4 w348 = RDW(1, 36864);
MMF(o8_0, 232, 235, w344, h2_0);
u32x4 h9_0; HLD4(h9_0, 0, 6144);
MMF(o8_1, 236, 239, w344, h2_1);
SB;
const u32x4 w349 = RDW(1, 38912);
HWAIT4(25, h3_0, h3_1);
MMF(o0_0, 184, 187, w345, h3_0);
MMF(o0_1, 188, 191, w345, h3_1);
SB;
PRIO(0);
const u32x4 w350 = RDW(0, 0);
MMF(o1_0, 192, 195, w346, h3_0);
u32x4 h9_1; HLD4(h9_1, 1, 6144);
MMF(o1_1, 196, 199, w346, h3_1);
SB;
const u32x4 w351 = RDW(0, 2048);
MMF(o2_0, 200, 203, w347, h3_0);
MMF(o2_1, 204, 207, w347, h3_1);
SB;
SYNC(20);
PRIO(3);
const u32x4 w352 = RDW(0, 4096);
MMF(o3_0, 208, 211, w348, h3_0);
DMA(sWd, 131072, vL16, 49152);
MMF(o3_1, 212, 215, w348, h3_1);
SB;
const u32x4 w353 = RDW(0, 6144);
MMF(o4_0, 216, 219, w349, h3_0);
MMF(o4_1, 220, 223, w349, h3_1);
SB;
const u32x4 w354 = RDW(0, 8192);
MMF(o5_0, 224, 227, w350, h3_0);
DMA(sWd, 139264, vL16, 57344);
MMF(o5_1, 228, 231, w350, h3_1);
SB;
const u32x4 w355 = RDW(0, 10240);
MMF(o8_0, 232, 235, w351, h3_0);
FLAG(6, 3, sFw);
MMF(o8_1, 236, 239, w351, h3_1);
SB;
const u32x4 w356 = RDW(0, 12288);
HWAIT4(21, h4_0, h4_1);
MMF(o0_0, 184, 187, w352, h4_0);
DMA(sWd, 147456, vL16, 131072);
MMF(o0_1, 188, 191, w352, h4_1);
SB;
const u32x4 w357 = RDW(0, 14336);
MMF(o1_0, 192, 195, w353, h4_0);
MMF(o1_1, 196, 199, w353, h4_1);
SB;
PRIO(2);
const u32x4 w358 = RDW(1, 40960);
MMF(o2_0, 200, 203, w354, h4_0);
DMA(sWd, 155648, vL16, 65536);
MMF(o2_1, 204, 207, w354, h4_1);
SB;
const u32x4 w359 = RDW(1, 43008);
MMF(o3_0, 208, 211, w355, h4_0);
MMF(o3_1, 212, 215, w355, h4_1);
SB;
const u32x4 w360 = RDW(1, 45056);
MMF(o4_0, 216, 219, w356, h4_0);
DMA(sWd, 163840, vL16, 73728);
MMF(o4_1, 220, 223, w356, h4_1);
SB;
const u32x4 w361 = RDW(1, 47104);
MMF(o5_0, 224, 227, w357, h4_0);
MMF(o5_1, 228, 231, w357, h4_1);
SB;
const u32x4 w362 = RDW(1, 49152);
MMF(o8_0, 232, 235, w358, h4_0);
DMA(sWd, 172032, vL16, 139264);
MMF(o8_1, 236, 239, w358, h4_1);
SB;
const u32x4 w363 = RDW(1, 51200);
HWAIT4(22, h5_0, h5_1);
MMF(o0_0, 184, 187, w359, h5_0);
MMF(o0_1, 188, 191, w359, h5_1);
SB;
PRIO(1);
const u32x4 w364 = RDW(1, 53248);
MMF(o1_0, 192, 195, w360, h5_0);
u32x4 h10_0; HLD4(h10_0, 0, 11264);
MMF(o1_1, 196, 199, w360, h5_1);
SB;
const u32x4 w365 = RDW(1, 55296);
MMF(o2_0, 200, 203, w361, h5_0);
MMF(o2_1, 204, 207, w361, h5_1);
SB;
const u32x4 w366 = RDW(0, 32768);
MMF(o3_0, 208, 211, w362, h5_0);
u32x4 h10_1; HLD4(h10_1, 1, 11264);
MMF(o3_1, 212, 215, w362, h5_1);
SB;
const u32x4 w367 = RDW(0, 34816);
MMF(o4_0, 216, 219, w363, h5_0);
MMF(o4_1, 220, 223, w363, h5_1);
SB;
const u32x4 w368 = RDW(0, 36864);
MMF(o5_0, 224, 227, w364, h5_0);
u32x4 h11_0; HLD4(h11_0, 0, 15360);
MMF(o5_1, 228, 231, w364, h5_1);
SB;
const u32x4 w369 = RDW(0, 38912);
MMF(o8_0, 232, 235, w365, h5_0);
MMF(o8_1, 236, 239, w365, h5_1);
SB;
const u32x4 w370 = RDW(0, 40960);
HWAIT4(23, h6_0, h6_1);
MMF(o0_0, 184, 187, w366, h6_0);
u32x4 h11_1; HLD4(h11_1, 1, 15360);
MMF(o0_1, 188, 191, w366, h6_1);
SB;
PRIO(0);
const u32x4 w371 = RDW(0, 43008);
MMF(o1_0, 192, 195, w367, h6_0);
MMF(o1_1, 196, 199, w367, h6_1);
SB;
const u32x4 w372 = RDW(0, 45056);
MMF(o2_0, 200, 203, w368, h6_0);
DMA(sWd, 180224, vL16, 81920);
MMF(o2_1, 204, 207, w368, h6_1);
SB;
SYNC(10);
PRIO(3);
const u32x4 w373 = RDW(0, 47104);
MMF(o3_0, 208, 211, w369, h6_0);
POLL(sFr, vL16, 8);
MMF(o3_1, 212, 215, w369, h6_1);
SB;
const u32x4 w374 = RDW(1, 57344);
MMF(o4_0, 216, 219, w370, h6_0);
DMA(sWd, 188416, vL16, 90112);
MMF(o4_1, 220, 223, w370, h6_1);
SB;
const u32x4 w375 = RDW(1, 59392);
MMF(o5_0, 224, 227, w371, h6_0);
MMF(o5_1, 228, 231, w371, h6_1);
SB;
const u32x4 w376 = RDW(1, 61440);
MMF(o8_0, 232, 235, w372, h6_0);
DMA(sWd, 196608, vL16, 147456);
MMF(o8_1, 236, 239, w372, h6_1);
SB;
const u32x4 w377 = RDW(1, 63488);
HWAIT4(23, h7_0, h7_1);
MMF(o0_0, 184, 187, w373, h7_0);
MMF(o0_1, 188, 191, w373, h7_1);
SB;
PRIO(2);
const u32x4 w378 = RDW(0, 16384);
MMF(o1_0, 192, 195, w374, h7_0);
DMA(sWd, 204800, vL16, 98304);
MMF(o1_1, 196, 199, w374, h7_1);
SB;
const u32x4 w379 = RDW(0, 18432);
MMF(o2_0, 200, 203, w375, h7_0);
MMF(o2_1, 204, 207, w375, h7_1);
SB;
const u32x4 w380 = RDW(0, 20480);
MMF(o3_0, 208, 211, w376, h7_0);
DMA(sWd, 212992, vL16, 0);
MMF(o3_1, 212, 215, w376, h7_1);
SB;
const u32x4 w381 = RDW(0, 22528);
MMF(o4_0, 216, 219, w377, h7_0);
CMP(4, 3);
MMF(o4_1, 220, 223, w377, h7_1);
SB;
const u32x4 w382 = RDW(0, 24576);
MMF(o5_0, 224, 227, w378, h7_0);
DMA(sWd, 221184, vL16, 8192);
MMF(o5_1, 228, 231, w378, h7_1);
SB;
PRIO(1);
const u32x4 w383 = RDW(0, 26624);
MMF(o8_0, 232, 235, w379, h7_0);
CHECK(3, sFr, vL16, 8);
MMF(o8_1, 236, 239, w379, h7_1);
SB;
const u32x4 w384 = RDW(0, 28672);
HWAIT4(20, h8_0, h8_1);
MMF(o0_0, 184, 187, w380, h8_0);
u32x4 h12_0; HLD4(h12_0, 0, 3072);
MMF(o0_1, 188, 191, w380, h8_1);
SB;
const u32x4 w385 = RDW(0, 30720);
MMF(o1_0, 192, 195, w381, h8_0);
MMF(o1_1, 196, 199, w381, h8_1);
SB;
const u32x4 w386 = RDW(0, 49152);
MMF(o2_0, 200, 203, w382, h8_0);
u32x4 h12_1; HLD4(h12_1, 1, 3072);
MMF(o2_1, 204, 207, w382, h8_1);
SB;
const u32x4 w387 = RDW(0, 51200);
MMF(o3_0, 208, 211, w383, h8_0);
MMF(o3_1, 212, 215, w383, h8_1);
SB;
const u32x4 w388 = RDW(0, 53248);
MMF(o4_0, 216, 219, w384, h8_0);
u32x4 h13_0; HLD4(h13_0, 0, 7168);
MMF(o4_1, 220, 223, w384, h8_1);
SB;
PRIO(0);
const u32x4 w389 = RDW(0, 55296);
MMF(o5_0, 224, 227, w385, h8_0);
MMF(o5_1, 228, 231, w385, h8_1);
SB;
const u32x4 w390 = RDW(0, 57344);
MMF(o8_0, 232, 235, w386, h8_0);
u32x4 h13_1; HLD4(h13_1, 1, 7168);
MMF(o8_1, 236, 239, w386, h8_1);
SB;
SYNC(15);
PRIO(3);
const u32x4 w391 = RDW(0, 59392);
HWAIT4(22, h9_0, h9_1);
MMF(o0_0, 184, 187, w387, h9_0);
MMF(o0_1, 188, 191, w387, h9_1);
SB;
const u32x4 w392 = RDW(0, 61440);
MMF(o1_0, 192, 195, w388, h9_0);
u32x4 h14_0; HLD4(h14_0, 0, 12288);
MMF(o1_1, 196, 199, w388, h9_1);
SB;
const u32x4 w393 = RDW(0, 63488);
MMF(o2_0, 200, 203, w389, h9_0);
MMF(o2_1, 204, 207, w389, h9_1);
SB;
const u32x4 w394 = RDW(2, 0);
MMF(o3_0, 208, 211, w390, h9_0);
u32x4 h14_1; HLD4(h14_1, 1, 12288);
MMF(o3_1, 212, 215, w390, h9_1);
SB;
const u32x4 w395 = RDW(2, 2048);
MMF(o4_0, 216, 219, w391, h9_0);
MMF(o4_1, 220, 223, w391, h9_1);
SB;
PRIO(2);
const u32x4 w396 = RDW(2, 4096);
MMF(o5_0, 224, 227, w392, h9_0);
DMA(sWd, 229376, vL16, 106496);
MMF(o5_1, 228, 231, w392, h9_1);
SB;
const u32x4 w397 = RDW(2, 6144);
MMF(o8_0, 232, 235, w393, h9_0);
MMF(o8_1, 236, 239, w393, h9_1);
SB;
const u32x4 w398 = RDW(1, 0);
HWAIT4(16, h10_0, h10_1);
MMF(o0_0, 184, 187, w394, h10_0);
DMA(sWd, 237568, vL16, 114688);
MMF(o0_1, 188, 191, w394, h10_1);
SB;
const u32x4 w399 = RDW(1, 2048);
MMF(o1_0, 192, 195, w395, h10_0);
MMF(o1_1, 196, 199, w395, h10_1);
SB;
const u32x4 w400 = RDW(1, 4096);
MMF(o2_0, 200, 203, w396, h10_0);
DMA(sWd, 245760, vL16, 32768);
MMF(o2_1, 204, 207, w396, h10_1);
SB;
PRIO(1);
const u32x4 w401 = RDW(1, 6144);
MMF(o3_0, 208, 211, w397, h10_0);
MMF(o3_1, 212, 215, w397, h10_1);
SB;
const u32x4 w402 = RDW(1, 8192);
MMF(o4_0, 216, 219, w398, h10_0);
DMA(sWd, 253952, vL16, 40960);
MMF(o4_1, 220, 223, w398, h10_1);
SB;
const u32x4 w403 = RDW(1, 10240);
MMF(o5_0, 224, 227, w399, h10_0);
MMF(o5_1, 228, 231, w399, h10_1);
SB;
const u32x4 w404 = RDW(1, 12288);
MMF(o8_0, 232, 235, w400, h10_0);
u32x4 h15_0; HLD4(h15_0, 0, 16384);
MMF(o8_1, 236, 239, w400, h10_1);
SB;
const u32x4 w405 = RDW(1, 14336);
HWAIT4(18, h11_0, h11_1);
MMF(o0_0, 184, 187, w401, h11_0);
MMF(o0_1, 188, 191, w401, h11_1);
SB;
const u32x4 w406 = RDW(2, 8192);
MMF(o1_0, 192, 195, w402, h11_0);
u32x4 h15_1; HLD4(h15_1, 1, 16384);
MMF(o1_1, 196, 199, w402, h11_1);
SB;
PRIO(0);
const u32x4 w407 = RDW(2, 10240);
MMF(o2_0, 200, 203, w403, h11_0);
MMF(o2_1, 204, 207, w403, h11_1);
SB;
const u32x4 w408 = RDW(2, 12288);
MMF(o3_0, 208, 211, w404, h11_0);
u32x2 h16_0a, h16_0b; HLD2(h16_0a, 0, 8192); HLD2(h16_0b, 0, 8704);
MMF(o3_1, 212, 215, w404, h11_1);
SB;
SYNC(15);
PRIO(3);
const u32x4 w409 = RDW(2, 14336);
MMF(o4_0, 216, 219, w405, h11_0);
MMF(o4_1, 220, 223, w405, h11_1);
SB;
const u32x4 w410 = RDW(1, 16384);
MMF(o5_0, 224, 227, w406, h11_0);
u32x2 h16_1a, h16_1b; HLD2(h16_1a, 1, 8192); HLD2(h16_1b, 1, 8704);
MMF(o5_1, 228, 231, w406, h11_1);
SB;
const u32x4 w411 = RDW(1, 18432);
MMF(o8_0, 232, 235, w407, h11_0);
MMF(o8_1, 236, 239, w407, h11_1);
SB;
const u32x4 w412 = RDW(1, 20480);
HWAIT4(14, h12_0, h12_1);
MMF(o0_0, 184, 187, w408, h12_0);
DMA(sWd, 262144, vL16, 122880);
MMF(o0_1, 188, 191, w408, h12_1);
SB;
const u32x4 w413 = RDW(1, 22528);
MMF(o1_0, 192, 195, w409, h12_0);
MMF(o1_1, 196, 199, w409, h12_1);
SB;
PRIO(2);
const u32x4 w414 = RDW(1, 24576);
MMF(o2_0, 200, 203, w410, h12_0);
DMA(sWd, 270336, vL16, 16384);
MMF(o2_1, 204, 207, w410, h12_1);
SB;
const u32x4 w415 = RDW(1, 26624);
MMF(o3_0, 208, 211, w411, h12_0);
MMF(o3_1, 212, 215, w411, h12_1);
SB;
const u32x4 w416 = RDW(1, 28672);
MMF(o4_0, 216, 219, w412, h12_0);
u32x2 h17_0a, h17_0b; HLD2(h17_0a, 0, 17408); HLD2(h17_0b, 0, 17920);
MMF(o4_1, 220, 223, w412, h12_1);
SB;
const u32x4 w417 = RDW(1, 30720);
MMF(o5_0, 224, 227, w413, h12_0);
MMF(o5_1, 228, 231, w413, h12_1);
SB;
const u32x4 w418 = RDW(2, 16384);
MMF(o8_0, 232, 235, w414, h12_0);
u32x2 h17_1a, h17_1b; HLD2(h17_1a, 1, 17408); HLD2(h17_1b, 1, 17920);
MMF(o8_1, 236, 239, w414, h12_1);
SB;
PRIO(1);
const u32x4 w419 = RDW(2, 18432);
HWAIT4(18, h13_0, h13_1);
MMF(o0_0, 184, 187, w415, h13_0);
MMF(o0_1, 188, 191, w415, h13_1);
SB;
const u32x4 w420 = RDW(2, 20480);
MMF(o1_0, 192, 195, w416, h13_0);
DMA(sWd, 278528, vL16, 24576);
MMF(o1_1, 196, 199, w416, h13_1);
SB;
const u32x4 w421 = RDW(2, 22528);
MMF(o2_0, 200, 203, w417, h13_0);
MMF(o2_1, 204, 207, w417, h13_1);
SB;
const u32x4 w422 = RDW(1, 32768);
MMF(o3_0, 208, 211, w418, h13_0);
DMA(sWd, 286720, vL16, 49152);
MMF(o3_1, 212, 215, w418, h13_1);
SB;
const u32x4 w423 = RDW(1, 34816);
MMF(o4_0, 216, 219, w419, h13_0);
MMF(o4_1, 220, 223, w419, h13_1);
SB;
const u32x4 w424 = RDW(1, 36864);
MMF(o5_0, 224, 227, w420, h13_0);
DMA(sWd, 294912, vL16, 57344);
MMF(o5_1, 228, 231, w420, h13_1);
SB;
PRIO(0);
const u32x4 w425 = RDW(1, 38912);
MMF(o8_0, 232, 235, w421, h13_0);
MMF(o8_1, 236, 239, w421, h13_1);
SB;
const u32x4 w426 = RDW(0, 0);
HWAIT4(19, h14_0, h14_1);
MMF(o0_0, 184, 187, w422, h14_0);
DMA(sWd, 303104, vL16, 131072);
MMF(o0_1, 188, 191, w422, h14_1);
SB;
SYNC(16);
PRIO(3);
const u32x4 w427 = RDW(0, 2048);
MMF(o1_0, 192, 195, w423, h14_0);
POLL(sEr, vL16, 32);
MMF(o1_1, 196, 199, w423, h14_1);
SB;
const u32x4 w428 = RDW(0, 4096);
MMF(o2_0, 200, 203, w424, h14_0);
DMA(sWd, 311296, vL16, 65536);
MMF(o2_1, 204, 207, w424, h14_1);
SB;
const u32x4 w429 = RDW(0, 6144);
MMF(o3_0, 208, 211, w425, h14_0);
MMF(o3_1, 212, 215, w425, h14_1);
SB;
const u32x4 w430 = RDW(0, 8192);
MMF(o4_0, 216, 219, w426, h14_0);
DMA(sWd, 319488, vL16, 73728);
MMF(o4_1, 220, 223, w426, h14_1);
SB;
const u32x4 w431 = RDW(0, 10240);
MMF(o5_0, 224, 227, w427, h14_0);
MMF(o5_1, 228, 231, w427, h14_1);
SB;
const u32x4 w432 = RDW(0, 12288);
MMF(o8_0, 232, 235, w428, h14_0);
DMA(sWd, 327680, vL16, 139264);
MMF(o8_1, 236, 239, w428, h14_1);
SB;
PRIO(2);
const u32x4 w433 = RDW(0, 14336);
HWAIT4(18, h15_0, h15_1);
MMF(o0_0, 184, 187, w429, h15_0);
MMF(o0_1, 188, 191, w429, h15_1);
SB;
const u32x4 w434 = RDW(1, 40960);
MMF(o1_0, 192, 195, w430, h15_0);
MMF(o1_1, 196, 199, w430, h15_1);
SB;
const u32x4 w435 = RDW(1, 43008);
MMF(o2_0, 200, 203, w431, h15_0);
MMF(o2_1, 204, 207, w431, h15_1);
SB;
const u32x4 w436 = RDW(1, 45056);
MMF(o3_0, 208, 211, w432, h15_0);
MMF(o3_1, 212, 215, w432, h15_1);
SB;
const u32x4 w437 = RDW(1, 47104);
MMF(o4_0, 216, 219, w433, h15_0);
MMF(o4_1, 220, 223, w433, h15_1);
SB;
const u32x4 w438 = RDW(1, 49152);
MMF(o5_0, 224, 227, w434, h15_0);
MMF(o5_1, 228, 231, w434, h15_1);
SB;
PRIO(1);
const u32x4 w439 = RDW(1, 51200);
MMF(o8_0, 232, 235, w435, h15_0);
CMP(3, 1);
MMF(o8_1, 236, 239, w435, h15_1);
SB;
const u32x4 w440 = RDW(1, 53248);
HWAIT2(14, h16_0a, h16_0b, h16_1a, h16_1b);
u32x4 h16_0 = CAT(h16_0a, h16_0b), h16_1 = CAT(h16_1a, h16_1b); SETTLE(h16_0, h16_1);
MMF(o0_0, 184, 187, w436, h16_0);
MMF(o0_1, 188, 191, w436, h16_1);
SB;
const u32x4 w441 = RDW(1, 55296);
MMF(o1_0, 192, 195, w437, h16_0);
CHECK(1, sEr, vL16, 32);
MMF(o1_1, 196, 199, w437, h16_1);
SB;
const u32x4 w442 = RDW(0, 32768);
MMF(o2_0, 200, 203, w438, h16_0);
MMF(o2_1, 204, 207, w438, h16_1);
SB;
const u32x4 w443 = RDW(0, 34816);
MMF(o3_0, 208, 211, w439, h16_0);
MMF(o3_1, 212, 215, w439, h16_1);
SB;
const u32x4 w444 = RDW(0, 36864);
MMF(o4_0, 216, 219, w440, h16_0);
MMF(o4_1, 220, 223, w440, h16_1);
SB;
PRIO(0);
const u32x4 w445 = RDW(0, 38912);
MMF(o5_0, 224, 227, w441, h16_0);
MMF(o5_1, 228, 231, w441, h16_1);
SB;
const u32x4 w446 = RDW(0, 40960);
MMF(o8_0, 232, 235, w442, h16_0);
MMF(o8_1, 236, 239, w442, h16_1);
SB;
SYNC(12);
PRIO(3);
const u32x4 w447 = RDW(0, 43008);
HWAIT2(8, h17_0a, h17_0b, h17_1a, h17_1b);
u32x4 h17_0 = CAT(h17_0a, h17_0b), h17_1 = CAT(h17_1a, h17_1b); SETTLE(h17_0, h17_1);
MMF(o0_0, 184, 187, w443, h17_0);
MMF(o0_1, 188, 191, w443, h17_1);
SB;
const u32x4 w448 = RDW(0, 45056);
MMF(o1_0, 192, 195, w444, h17_0);
EPOCH();
MMF(o1_1, 196, 199, w444, h17_1);
SB;
PRIO(2);
const u32x4 w449 = RDW(0, 47104);
MMF(o2_0, 200, 203, w445, h17_0);
XW(o0_1, 1, 16384);
MMF(o2_1, 204, 207, w445, h17_1);
SB;
const u32x4 w450 = RDW(1, 57344);
MMF(o3_0, 208, 211, w446, h17_0);
XW(o1_1, 1, 24576);
MMF(o3_1, 212, 215, w446, h17_1);
SB;
const u32x4 w451 = RDW(1, 59392);
MMF(o4_0, 216, 219, w447, h17_0);
XW(o2_1, 2, 16384);
MMF(o4_1, 220, 223, w447, h17_1);
SB;
PRIO(1);
const u32x4 w452 = RDW(1, 61440);
MMF(o5_0, 224, 227, w448, h17_0);
XW(o3_1, 1, 32768);
MMF(o5_1, 228, 231, w448, h17_1);
SB;
const u32x4 w453 = RDW(1, 63488);
MMF(o8_0, 232, 235, w449, h17_0);
XW(o4_1, 0, 0);
MMF(o8_1, 236, 239, w449, h17_1);
SB;

const u32x4 w454 = RDW(0, 16384);
f32x4 o6_0; MMF0(o6_0, 240, 243, w450, h0_0);
XW(o5_1, 0, 8192);
f32x4 o6_1; MMF0(o6_1, 244, 247, w450, h0_1);
SB;
PRIO(0);
const u32x4 w455 = RDW(0, 18432);
f32x4 o7_0; MMF0(o7_0, 248, 251, w451, h0_0);
XW(o8_1, 1, 40960);
f32x4 o7_1; MMF0(o7_1, 252, 255, w451, h0_1);
SB;
const u32x4 w456 = RDW(0, 20480);
MMF(o6_0, 240, 243, w452, h1_0);
MMF(o6_1, 244, 247, w452, h1_1);
SB;
SYNCL(1);
PRIO(3);
const u32x4 w457 = RDW(0, 22528);
MMF(o7_0, 248, 251, w453, h1_0);
const f32x4 r0 = XR(1, 16384);
const f32x4 r1 = XR(1, 24576);
MMF(o7_1, 252, 255, w453, h1_1);
SB;
const u32x4 w458 = RDW(0, 24576);
MMF(o6_0, 240, 243, w454, h2_0);
const f32x4 r2 = XR(2, 16384);
const f32x4 r3 = XR(1, 32768);
MMF(o6_1, 244, 247, w454, h2_1);
SB;
const u32x4 w459 = RDW(0, 26624);
MMF(o7_0, 248, 251, w455, h2_0);
ADD4(o0_0, 184, 185, 186, 187, r0);
const f32x4 r4 = XR(0, 0);
const f32x4 r5 = XR(0, 8192);
MMF(o7_1, 252, 255, w455, h2_1);
SB;
const u32x4 w460 = RDW(0, 28672);
MMF(o6_0, 240, 243, w456, h3_0);
const u32x2 t0 = PACK(o0_0);
ADD4(o1_0, 192, 193, 194, 195, r1);
const f32x4 r8 = XR(1, 40960);
MMF(o6_1, 244, 247, w456, h3_1);
SB;
const u32x4 w461 = RDW(0, 30720);
MMF(o7_0, 248, 251, w457, h3_0);
const u32x2 t1 = PACK(o1_0);
SEND4(0, t0, t1);
MMF(o7_1, 252, 255, w457, h3_1);
SB;
const u32x4 w462 = RDW(0, 49152);
MMF(o6_0, 240, 243, w458, h4_0);
ADD4(o2_0, 200, 201, 202, 203, r2);
MMF(o6_1, 244, 247, w458, h4_1);
SB;
const u32x4 w463 = RDW(0, 51200);
MMF(o7_0, 248, 251, w459, h4_0);
const u32x2 t2 = PACK(o2_0);
ADD4(o3_0, 208, 209, 210, 211, r3);
MMF(o7_1, 252, 255, w459, h4_1);
SB;
const u32x4 w464 = RDW(0, 53248);
MMF(o6_0, 240, 243, w460, h5_0);
const u32x2 t3 = PACK(o3_0);
SEND4(1, t2, t3);
MMF(o6_1, 244, 247, w460, h5_1);
SB;
const u32x4 w465 = RDW(0, 55296);
MMF(o7_0, 248, 251, w461, h5_0);
ADD4(o4_0, 216, 217, 218, 219, r4);
MMF(o7_1, 252, 255, w461, h5_1);
SB;
const u32x4 w466 = RDW(0, 57344);
MMF(o6_0, 240, 243, w462, h6_0);
const u32x2 t4 = PACK(o4_0);
ADD4(o5_0, 224, 225, 226, 227, r5);
MMF(o6_1, 244, 247, w462, h6_1);
SB;
PRIO(2);
const u32x4 w467 = RDW(0, 59392);
MMF(o7_0, 248, 251, w463, h6_0);
const u32x2 t5 = PACK(o5_0);
SEND4(2, t4, t5);
MMF(o7_1, 252, 255, w463, h6_1);
SB;
const u32x4 w468 = RDW(0, 61440);
MMF(o6_0, 240, 243, w464, h7_0);
ADD4(o8_0, 232, 233, 234, 235, r8);
MMF(o6_1, 244, 247, w464, h7_1);
SB;
const u32x4 w469 = RDW(0, 63488);
MMF(o7_0, 248, 251, w465, h7_0);
const u32x2 t8 = PACK(o8_0);
SEND2(t8);
MMF(o7_1, 252, 255, w465, h7_1);
SB;
const u32x4 w470 = RDW(2, 0);
MMF(o6_0, 240, 243, w466, h8_0);
MMF(o6_1, 244, 247, w466, h8_1);
SB;
const u32x4 w471 = RDW(2, 2048);
MMF(o7_0, 248, 251, w467, h8_0);
MMF(o7_1, 252, 255, w467, h8_1);
SB;
const u32x4 w472 = RDW(2, 4096);
MMF(o6_0, 240, 243, w468, h9_0);
MMF(o6_1, 244, 247, w468, h9_1);
SB;
const u32x4 w473 = RDW(2, 6144);
MMF(o7_0, 248, 251, w469, h9_0);
MMF(o7_1, 252, 255, w469, h9_1);
SB;
const u32x4 w474 = RDW(1, 0);
MMF(o6_0, 240, 243, w470, h10_0);
MMF(o6_1, 244, 247, w470, h10_1);
SB;
const u32x4 w475 = RDW(1, 2048);
MMF(o7_0, 248, 251, w471, h10_0);
MMF(o7_1, 252, 255, w471, h10_1);
SB;
const u32x4 w476 = RDW(1, 4096);
MMF(o6_0, 240, 243, w472, h11_0);
MMF(o6_1, 244, 247, w472, h11_1);
SB;
PRIO(1);
const u32x4 w477 = RDW(1, 6144);
MMF(o7_0, 248, 251, w473, h11_0);
MMF(o7_1, 252, 255, w473, h11_1);
SB;
const u32x4 w478 = RDW(1, 8192);
MMF(o6_0, 240, 243, w474, h12_0);
MMF(o6_1, 244, 247, w474, h12_1);
SB;
const u32x4 w479 = RDW(1, 10240);
MMF(o7_0, 248, 251, w475, h12_0);
MMF(o7_1, 252, 255, w475, h12_1);
SB;
const u32x4 w480 = RDW(1, 12288);
MMF(o6_0, 240, 243, w476, h13_0);
MMF(o6_1, 244, 247, w476, h13_1);
SB;
const u32x4 w481 = RDW(1, 14336);
MMF(o7_0, 248, 251, w477, h13_0);
MMF(o7_1, 252, 255, w477, h13_1);
SB;
const u32x4 w482 = RDW(2, 8192);
MMF(o6_0, 240, 243, w478, h14_0);
MMF(o6_1, 244, 247, w478, h14_1);
SB;
const u32x4 w483 = RDW(2, 10240);
MMF(o7_0, 248, 251, w479, h14_0);
MMF(o7_1, 252, 255, w479, h14_1);
SB;
const u32x4 w484 = RDW(2, 12288);
MMF(o6_0, 240, 243, w480, h15_0);
MMF(o6_1, 244, 247, w480, h15_1);
SB;
const u32x4 w485 = RDW(2, 14336);
MMF(o7_0, 248, 251, w481, h15_0);
INBOX_LD();
MMF(o7_1, 252, 255, w481, h15_1);
SB;
MMF(o6_0, 240, 243, w482, h16_0);
MMF(o6_1, 244, 247, w482, h16_1);
SB;
MMF(o7_0, 248, 251, w483, h16_0);
MMF(o7_1, 252, 255, w483, h16_1);
SB;
PRIO(0);
MMF(o6_0, 240, 243, w484, h17_0);
MMF(o6_1, 244, 247, w484, h17_1);
SB;
MMF(o7_0, 248, 251, w485, h17_0);
MMF(o7_1, 252, 255, w485, h17_1);
SB;

XPRE();
XW(o6_1, 1, 49152);
XW(o7_1, 0, 32768);
XSYNC();
o6_0 += XR(1, 49152);
o7_0 += XR(0, 32768);

  OPAQUE(vL16);
  const int ln = vL16 >> 4;
  const float var = fmaxf(*(LDS float*)(ldsp(sm0 + M_RS + (32 * rg + 16 * kg + (ln & 15)) * 4)), 0.f);
  const float rs1 = __builtin_amdgcn_rcpf(rbf(__builtin_amdgcn_sqrtf(rbf(rbf(var) + 1e-5f))));
  float rs2 = rs1 * rs1;
  auto un = [&](u32 a, u32 b) { return f32x4{bflo(a), bfhi(a), bflo(b), bfhi(b)}; };
  f32x4 lo = o6_0 * rs2 + un(res[0], res[1]), hi = o7_0 * rs2 + un(res[2], res[3]), t8f = o8_0 * rs2 + un(res8[0], res8[1]);
  u32 oa = (tok0 + (ln & 15)) * (SD * 2) + (32 * (4 * kap + cc) + 8 * (ln >> 4)) * 2;
  u32 ob = rg == kap ? (tok0 + (ln & 15)) * (SD * 2) + (512 + 16 * cc + 4 * (ln >> 4)) * 2 : 1u << 30;
  OPAQUE(lo); OPAQUE(hi); OPAQUE(t8f); OPAQUE(oa); OPAQUE(ob); OPAQUE(rs2);
  auto landed = [&]() { asm volatile("s_waitcnt vmcnt(0)" : "+v"(q[0]), "+v"(q[1]), "+v"(q[2]), "+v"(q8[0]), "+v"(q8[1]), "+v"(q8[2]) :: "memory"); };
  auto empty = [&]() {
    u32 mx = 0;
#pragma unroll
    for (int j = 0; j < 3; ++j) mx = max(max(max(mx, q[j][0]), max(q[j][1], q[j][2])), max(max(q[j][3], q8[j][0]), q8[j][1]));
    return __any(mx == ~0u);
  };
  landed();
  if (empty()) {
    u32 sp = 0;
#pragma clang loop unroll(disable)
    do {
      __builtin_amdgcn_s_sleep(2);
      INBOX_LD();
      landed();
    } while (empty() && ++sp < (1u << 20));
  }
  const auto rso = __builtin_amdgcn_make_buffer_rsrc(out, 0, SL * SD * 2, 0x00020000);
  lo += ((un(q[0][0], q[0][1]) + un(q[1][0], q[1][1])) + un(q[2][0], q[2][1])) * rs2;
  hi += ((un(q[0][2], q[0][3]) + un(q[1][2], q[1][3])) + un(q[2][2], q[2][3])) * rs2;
  __builtin_amdgcn_raw_buffer_store_b128(pk8(lo, hi), rso, oa, 0, 16);
  SB;
  t8f += ((un(q8[0][0], q8[0][1]) + un(q8[1][0], q8[1][1])) + un(q8[2][0], q8[2][1])) * rs2;
  __builtin_amdgcn_raw_buffer_store_b64(u32x2{pkbf(t8f[0], t8f[1]), pkbf(t8f[2], t8f[3])}, rso, ob, 0, 16);

  asm volatile(".subsection 2\n\t.fill 256, 4, 0xBF800000\n\t.subsection 0");
}

constexpr int A_LDS = F_LDS > M_LDS ? F_LDS : M_LDS;
__global__ __launch_bounds__(512, 2) void k_block(const u16* x, const u16* logA, const u16* Aim, const u16* Cre, const u16* Cim, const u16* logdt,
                                                  const u16* Dsk, const u16* wg, const u16* wu, const u16* wd, char* ws, u16* out) {
  mlp_phase(ws, wd, out, front_phase(x, logA, Aim, Cre, Cim, logdt, Dsk, wg, wu, wd, ws));
}

extern "C" void kernel_launch(void* const* d_in, const int* in_sizes, int n_in, void* d_out, int out_size, void* d_ws, size_t ws_size, hipStream_t stream) {
  (void)hipFuncSetAttribute((const void*)k_block, hipFuncAttributeMaxDynamicSharedMemorySize, A_LDS);
  k_block<<<256, 512, A_LDS, stream>>>((const u16*)d_in[0], (const u16*)d_in[1], (const u16*)d_in[2], (const u16*)d_in[3], (const u16*)d_in[4],
      (const u16*)d_in[5], (const u16*)d_in[6], (const u16*)d_in[8], (const u16*)d_in[9], (const u16*)d_in[10], (char*)d_ws, (u16*)d_out);
}
```
